```python
import torch

def _softmax(x, axis=-1):
    return torch.softmax(x, dim=axis)
def _gelu(x):
    return torch.nn.functional.gelu(x, approximate="tanh")
def _silu(x):
    return torch.nn.functional.silu(x)
def _sigmoid(x):
    return torch.sigmoid(x)
def _layernorm(x, axis=-1, eps=1e-5):
    m = x.mean(dim=axis, keepdim=True)
    v = x.var(dim=axis, keepdim=True, unbiased=False)
    return (x - m) / torch.sqrt(v + eps)
def _scatter_add(tgt, idx, src, axis=0):
    return tgt.clone().index_add_(axis, idx, src)
def _segment_sum(src, seg, n):
    out = torch.zeros((n,) + src.shape[1:], dtype=src.dtype)
    return out.index_add_(0, seg, src)
def _pad(x, pw):
    flat = tuple(v for pair in reversed(pw) for v in pair)
    return torch.nn.functional.pad(x, flat)

kernel_name = 'parallel_sum__grouped_kv_mqa__dense_activation_relu__prompt'

def setup_inputs():

    _w = (lambda a: (lambda t: t.to(torch.bfloat16) if t.is_floating_point() else t)(torch.as_tensor(a)))
    def _A_setup():
        B, S, D, H = 1, 2048, 640, 32
        Dh = D // H
        return {
            "x": _w((lambda *s: torch.randn(*s, dtype=torch.bfloat16) * 0.05)(B, S, D)),
            "wq": _w((lambda *s: torch.randn(*s, dtype=torch.bfloat16) * 0.1)(D, H * Dh)),
            "wk": _w((lambda *s: torch.randn(*s, dtype=torch.bfloat16) * 0.1)(D, Dh)),
            "wv": _w((lambda *s: torch.randn(*s, dtype=torch.bfloat16) * 0.05)(D, Dh)),
            "wo": _w((lambda *s: torch.randn(*s, dtype=torch.bfloat16) * 0.05)(H * Dh, D)),
        }
    _A_in = _A_setup()

    def _B_setup():
        B, S, D, Dff = 1, 2048, 640, 1728
        return {
            "x": _w((lambda *s: torch.randn(*s, dtype=torch.bfloat16) * 0.05)(B, S, D)),
            "w1": _w((lambda *s: torch.randn(*s, dtype=torch.bfloat16) * 0.05)(D, Dff)),
            "w2": _w((lambda *s: torch.randn(*s, dtype=torch.bfloat16) * 0.05)(Dff, D)),
        }
    _B_in = _B_setup()

    return {
        "A_x": _A_in["x"],
        "A_wq": _A_in["wq"],
        "A_wk": _A_in["wk"],
        "A_wv": _A_in["wv"],
        "A_wo": _A_in["wo"],
        "B_x": _B_in["x"],
        "B_w1": _B_in["w1"],
        "B_w2": _B_in["w2"],
    }

def reference(A_x, A_wq, A_wk, A_wv, A_wo, B_x, B_w1, B_w2):
    def _A_ref(x, wq, wk, wv, wo):
        B, S, D = x.shape
        H = 32
        Dh = D // H
        q = (lambda x, *d: x.permute(*d))((x @ wq).reshape(B, S, H, Dh), 0, 2, 1, 3)
        k = x @ wk
        v = x @ wv
        scores = torch.einsum("bhqd,bkd->bhqk", q, k) * (Dh ** -0.5)
        p = _softmax(scores, axis=-1)
        o = torch.einsum("bhqk,bkd->bhqd", p, v)
        return (lambda x, *d: x.permute(*d))(o, 0, 2, 1, 3).reshape(B, S, H * Dh) @ wo

    def _B_ref(x, w1, w2):
        h = torch.einsum("bsd,df->bsf", x, w1)
        zero = torch.zeros_like(h)
        h = torch.maximum(h, zero)
        return torch.einsum("bsf,fd->bsd", h, w2)

    x = A_x
    xn = _layernorm(x, axis=-1)
    return x + _A_ref(xn, A_wq, A_wk, A_wv, A_wo) + _B_ref(xn, B_w1, B_w2)
```

```cpp
#include <hip/hip_runtime.h>
#include <hip/hip_bf16.h>
typedef unsigned short u16;
__device__ __forceinline__ float bf2f(u16 v){ return __uint_as_float(((unsigned)v)<<16); }
__device__ __forceinline__ u16 f2bf(float f){ unsigned u=__float_as_uint(f); u += 0x7fff + ((u>>16)&1); return (u16)(u>>16); }
constexpr int S=2048, D=640, H=32, DH=20, FF=1728;

__global__ void ln_k(const u16* x, u16* xn){
  int row=blockIdx.x; int t=threadIdx.x;
  float v[10]; float s=0;
  for(int i=0;i<10;i++){ v[i]=bf2f(x[row*D+t+64*i]); s+=v[i]; }
  for(int o=32;o>0;o>>=1) s+=__shfl_xor(s,o);
  float m=s/D; float q=0;
  for(int i=0;i<10;i++){ float d=v[i]-m; q+=d*d; }
  for(int o=32;o>0;o>>=1) q+=__shfl_xor(q,o);
  float r=rsqrtf(q/D+1e-5f);
  for(int i=0;i<10;i++) xn[row*D+t+64*i]=f2bf((v[i]-m)*r);
}
template<int MODE> __global__ void gemm_k(const u16* A,const u16* B,u16* C,float* Cf,int M,int N,int K,float scale){
  __shared__ float As[16][17], Bs[16][17];
  int tx=threadIdx.x&15, ty=threadIdx.x>>4; int r=blockIdx.y*16+ty, c=blockIdx.x*16+tx;
  float acc=0;
  for(int k0=0;k0<K;k0+=16){
    As[ty][tx]=bf2f(A[r*K+k0+tx]);
    Bs[ty][tx]= c<N? bf2f(B[(k0+ty)*N+c]):0.f;
    __syncthreads();
    for(int k=0;k<16;k++) acc+=As[ty][k]*Bs[k][tx];
    __syncthreads();
  }
  if(c<N){
    if(MODE==0) C[r*N+c]=f2bf(acc*scale);
    if(MODE==1) C[r*N+c]=f2bf(fmaxf(acc,0.f));
    if(MODE==2) Cf[r*N+c]=acc;
  }
}
__global__ void attn_k(const u16* q,const u16* k,const u16* v,u16* o){
  __shared__ float ks[256][DH], vs[256][DH];
  int g=blockIdx.x*256+threadIdx.x; int row=g/H, h=g%H;
  float qv[DH]; for(int d=0;d<DH;d++) qv[d]=bf2f(q[row*D+h*DH+d]);
  float m=-1e30f,l=0,acc[DH]; for(int d=0;d<DH;d++) acc[d]=0;
  for(int k0=0;k0<S;k0+=256){
    __syncthreads();
    for(int d=0;d<DH;d++){ ks[threadIdx.x][d]=bf2f(k[(k0+threadIdx.x)*DH+d]); vs[threadIdx.x][d]=bf2f(v[(k0+threadIdx.x)*DH+d]); }
    __syncthreads();
    for(int j=0;j<256;j++){
      float s=0; for(int d=0;d<DH;d++) s+=qv[d]*ks[j][d];
      float mn=fmaxf(m,s); float a=exp2f(m-mn); float p=exp2f(s-mn); p=bf2f(f2bf(p));
      l=l*a+p; for(int d=0;d<DH;d++) acc[d]=acc[d]*a+p*vs[j][d]; m=mn;
    }
  }
  for(int d=0;d<DH;d++) o[row*D+h*DH+d]=f2bf(acc[d]/l);
}
__global__ void fin_k(const u16* x,const float* a,const float* b,u16* out){
  int i=blockIdx.x*256+threadIdx.x; out[i]=f2bf(bf2f(x[i])+a[i]+b[i]);
}
extern "C" void kernel_launch(void* const* d_in, const int* in_sizes, int n_in, void* d_out, int out_size, void* d_ws, size_t ws_size, hipStream_t stream){
  const u16* x=(const u16*)d_in[0]; const u16* wq=(const u16*)d_in[1]; const u16* wk=(const u16*)d_in[2]; const u16* wv=(const u16*)d_in[3]; const u16* wo=(const u16*)d_in[4];
  const u16* w1=(const u16*)d_in[6]; const u16* w2=(const u16*)d_in[7];
  char* ws=(char*)d_ws;
  u16* xn=(u16*)ws; u16* q=(u16*)(ws+(4<<20)); u16* k=(u16*)(ws+(8<<20)); u16* v=(u16*)(ws+(9<<20)); u16* h=(u16*)(ws+(10<<20)); u16* o=(u16*)(ws+(20<<20));
  float* fa=(float*)(ws+(24<<20)); float* fb=(float*)(ws+(32<<20));
  ln_k<<<S,64,0,stream>>>(x,xn);
  float c=1.4426950408889634f/sqrtf(20.f);
  gemm_k<0><<<dim3(D/16,S/16),256,0,stream>>>(xn,wq,q,nullptr,S,D,D,c);
  gemm_k<0><<<dim3(2,S/16),256,0,stream>>>(xn,wk,k,nullptr,S,DH,D,1.f);
  gemm_k<0><<<dim3(2,S/16),256,0,stream>>>(xn,wv,v,nullptr,S,DH,D,1.f);
  gemm_k<1><<<dim3(FF/16,S/16),256,0,stream>>>(xn,w1,h,nullptr,S,FF,D,1.f);
  attn_k<<<S*H/256,256,0,stream>>>(q,k,v,o);
  gemm_k<2><<<dim3(D/16,S/16),256,0,stream>>>(o,wo,nullptr,fa,S,D,D,1.f);
  gemm_k<2><<<dim3(D/16,S/16),256,0,stream>>>(h,w2,nullptr,fb,S,D,FF,1.f);
  fin_k<<<S*D/256,256,0,stream>>>(x,fa,fb,(u16*)d_out);
}
```

```cpp
#include <hip/hip_runtime.h>
#include <hip/hip_bf16.h>

typedef unsigned u32; typedef unsigned long long u64; typedef unsigned short u16;
typedef u32 u32x2 __attribute__((ext_vector_type(2)));
typedef u32 u32x4 __attribute__((ext_vector_type(4)));
typedef u32 u32x8 __attribute__((ext_vector_type(8)));
typedef float f32x2 __attribute__((ext_vector_type(2)));
typedef float f32x8 __attribute__((ext_vector_type(8)));
typedef float f32x16 __attribute__((ext_vector_type(16)));
typedef float f32x32 __attribute__((ext_vector_type(32)));
typedef short s16x4 __attribute__((ext_vector_type(4)));
typedef __bf16 bf16x2 __attribute__((ext_vector_type(2)));
typedef __bf16 bf16x8 __attribute__((ext_vector_type(8)));
#define LAS __attribute__((address_space(3)))
#define GAS __attribute__((address_space(1)))
#define DI __device__ __forceinline__

constexpr int PS = 34752;
constexpr int L_AR = 4 * PS;
constexpr int L_FL = L_AR + 4 * 6144;
constexpr int L_RS = 34048;
constexpr int LDS_BYTES = 163840;
constexpr int KVT = 2816;
constexpr size_t W_NONCE = 0, W_KVF = 32768, W_HF = 262144, W_OF = 294912, W_WFL = 49152, W_KV = 65536, W_O = 1u << 20, W_H = 4u << 20, W_WF = 12u << 20;
constexpr u32 CODE_LINES = 265;
constexpr u32 SPIN_CAP = 1u << 16;

DI u32 f2u(float f) { return __builtin_bit_cast(u32, f); }
DI float u2f(u32 u) { return __builtin_bit_cast(float, u); }
DI float hi_half(float v) { const u32 u = f2u(v); return u2f(__builtin_amdgcn_permlane32_swap(u, u, false, false)[1]); }
DI float mulf(float a, float b) { float r; asm("v_mul_f32 %0, %1, %2" : "=v"(r) : "v"(a), "v"(b)); return r; }
DI u32 rfl(u32 v) { return __builtin_amdgcn_readfirstlane(v); }
DI u32 pk2(float a, float b) { f32x2 v = {a, b}; return __builtin_bit_cast(u32, __builtin_convertvector(v, bf16x2)); }
DI u32x4 mksrd(const void* p, u32 bytes) { u64 a = (u64)p; u32x4 r = {(u32)a, (u32)(a >> 32) & 0xffffu, bytes, 0x00020000u}; return r; }
DI void st8(char* p, u64 v) { __hip_atomic_store((GAS u64*)p, v, __ATOMIC_RELAXED, __HIP_MEMORY_SCOPE_AGENT); }
DI void st16(char* base, u32 off, u32x4 v) { asm volatile("global_store_dwordx4 %0, %1, %2 sc1\n\ts_nop 1" :: "v"(off), "v"(v), "s"(base) : "memory"); }
DI void drain() { asm volatile("s_waitcnt vmcnt(0)" ::: "memory"); }
DI void ldrain() { asm volatile("s_waitcnt lgkmcnt(0)" ::: "memory"); }
template <int SL = 1> DI void lds_wait(LAS char* p, u32 want) {
#pragma unroll 1
  for (u32 i = 0; i < (SPIN_CAP << 4); ++i) { if (rfl(*(volatile LAS u32*)p) >= want) break; __builtin_amdgcn_s_sleep(SL); }
}
DI void lds_add(LAS char* p) { asm volatile("ds_add_u32 %0, %1" :: "v"((u32)(size_t)p), "v"(1u) : "memory"); }
DI void poll2(const char* base, u32 off, u64 epoch) {
#pragma unroll 1
  for (u32 i = 0; i < SPIN_CAP; ++i) {
    u32x4 f; asm volatile("global_load_dwordx4 %0, %1, %2 sc1\n\ts_waitcnt vmcnt(0)" : "=&v"(f) : "v"(off), "s"(base) : "memory");
    const bool ok = f[0] == (u32)epoch && f[1] == (u32)(epoch >> 32) && f[2] == (u32)epoch && f[3] == (u32)(epoch >> 32);
    if (__all(ok)) break;
    __builtin_amdgcn_s_sleep(1);
  }
}

DI void attn_exact(const char* kv, u32 vk0, u32 vk1, u32 vv, u32x4 q0, u32x2 q1, float rs, u32x4& frag, u32x2& left) {
  float m = -__builtin_inff();
  f32x16 o; for (int i = 0; i < 16; ++i) o[i] = 0.f;
#pragma unroll 1
  for (int t = 0; t < 64; ++t) {
    u32x4 k0, v0, v1; u32x2 k1; const char* p = kv + t * KVT;
    asm volatile("global_load_dwordx4 %0, %4, %7 sc1\n\tglobal_load_dwordx2 %1, %5, %7 sc1\n\tglobal_load_dwordx4 %2, %6, %7 sc1\n\t"
                 "global_load_dwordx4 %3, %6, %7 offset:768 sc1\n\ts_waitcnt vmcnt(0)"
                 : "=&v"(k0), "=&v"(k1), "=&v"(v0), "=&v"(v1) : "v"(vk0), "v"(vk1), "v"(vv), "s"(p) : "memory");
    f32x16 s; for (int i = 0; i < 16; ++i) s[i] = 0.f;
    s = __builtin_amdgcn_mfma_f32_32x32x8bf16_1k(__builtin_bit_cast(s16x4, k1), __builtin_bit_cast(s16x4, q1), s, 0, 0, 0);
    s = __builtin_amdgcn_mfma_f32_32x32x16_bf16(__builtin_bit_cast(bf16x8, k0), __builtin_bit_cast(bf16x8, q0), s, 0, 0, 0);
    float mx = m;
#pragma unroll
    for (int i = 0; i < 16; ++i) mx = fmaxf(mx, s[i]);
    mx = fmaxf(mx, __shfl_xor(mx, 32));
    float al = __builtin_amdgcn_exp2f(m - mx); m = mx;
    u32x4 pa, pb;
#pragma unroll
    for (int i = 0; i < 4; ++i) {
      pa[i] = pk2(__builtin_amdgcn_exp2f(s[2 * i] - mx), __builtin_amdgcn_exp2f(s[2 * i + 1] - mx));
      pb[i] = pk2(__builtin_amdgcn_exp2f(s[8 + 2 * i] - mx), __builtin_amdgcn_exp2f(s[9 + 2 * i] - mx));
    }
    asm volatile("s_nop 15" : "+v"(o), "+v"(al));
#pragma unroll
    for (int i = 0; i < 16; ++i) o[i] = mulf(o[i], al);
    asm volatile("s_nop 1" : "+v"(o));
    o = __builtin_amdgcn_mfma_f32_32x32x16_bf16(__builtin_bit_cast(bf16x8, v0), __builtin_bit_cast(bf16x8, pa), o, 0, 0, 0);
    o = __builtin_amdgcn_mfma_f32_32x32x16_bf16(__builtin_bit_cast(bf16x8, v1), __builtin_bit_cast(bf16x8, pb), o, 0, 0, 0);
  }
  asm volatile("s_nop 15" : "+v"(o));
  const float iv = 1.f / (hi_half(o[8]) * rs);
#pragma unroll
  for (int i = 0; i < 4; ++i) frag[i] = pk2(mulf(o[2 * i], iv), mulf(o[2 * i + 1], iv));
#pragma unroll
  for (int i = 0; i < 2; ++i) left[i] = pk2(mulf(o[8 + 2 * i], iv), mulf(o[9 + 2 * i], iv));
}

__global__ __launch_bounds__(512, 2) void fused_block_kernel(const u16* __restrict__ x, const u16* __restrict__ wq, const u16* __restrict__ wk,
    const u16* __restrict__ wv, const u16* __restrict__ wo, const u16* __restrict__ w1, const u16* __restrict__ w2, u16* __restrict__ out, char* __restrict__ ws) {
  __shared__ __attribute__((aligned(16))) char smem[LDS_BYTES];
  LAS char* lds = (LAS char*)smem;
  const u32 L0 = (u32)(size_t)lds;
  const int wave = rfl(threadIdx.x >> 6), q = wave & 3;
  const int b = blockIdx.x, jj = b >> 3, g = ((b & 7) >> 1) * 8 + (jj >> 2), c = (b & 1) * 4 + (jj & 3);
  const int ty = c & 1, pp = c >> 1;
  asm volatile("" : "+s"(x), "+s"(wq), "+s"(wk), "+s"(wv), "+s"(wo), "+s"(w1), "+s"(w2), "+s"(out), "+s"(ws));
  const int tid0 = threadIdx.x;
#define LANE_VARS int tid = tid0; asm volatile("" : "+v"(tid)); const int lane = tid & 63, r = lane & 31, hi = lane >> 5, g4 = lane >> 4, l15 = lane & 15, xrow = tid >> 3; \
  const u32 vo = (u32)xrow * 1280u + (u32)(tid & 7) * 16u;
  u64 cp; asm volatile("s_getpc_b64 %0" : "=s"(cp));
  const u32 cl = (u32)jj * 8u + wave, c2 = cl + 256u < CODE_LINES ? 32768u : 0u; cp = (cp & ~127ull) + (cl < CODE_LINES ? cl : 0u) * 128u;
  const u16* xg = x + (size_t)g * 64 * 640;
#define LN_ADDR const u32x4 ln = {L0 + (xrow >> 5) * 2 * PS + (tid & 7) * 512 + (((xrow & 31) ^ (tid & 7)) << 4), L0 + L_RS + xrow * 4, \
                                  L0 + L_FL + 64 + wave * 8, L0 + L_FL + 64 + (lane & 7) * 8};
#define XB_ADDR const u32 trl = ((lane >> 2) & 3) * 64 + ((lane >> 4) & 1) * 32 + (lane & 3) * 8;       \
  u32x8 xbv; _Pragma("unroll") for (int m = 0; m < 4; ++m) { xbv[m] = L0 + hi * 512 + ((r ^ ((2 * m + hi) & 7)) << 4); xbv[4 + m] = xbv[m] + 2 * PS; }

  asm volatile(
    "s_cmp_gt_u32 %[wave], 3\n"
    "s_cbranch_scc1 XL%=\n"
    "global_load_dwordx4 v[0:3], %[vo], %[xb] offset:0\n"
    "global_load_dwordx4 v[4:7], %[vo], %[xb] offset:128\n"
    "global_load_dwordx4 v[8:11], %[vo], %[xb] offset:256\n"
    "global_load_dwordx4 v[12:15], %[vo], %[xb] offset:384\n"
    "global_load_dwordx4 v[16:19], %[vo], %[xb] offset:512\n"
    "global_load_dwordx4 v[20:23], %[vo], %[xb] offset:640\n"
    "global_load_dwordx4 v[24:27], %[vo], %[xb] offset:768\n"
    "global_load_dwordx4 v[28:31], %[vo], %[xb] offset:896\n"
    "global_load_dwordx4 v[32:35], %[vo], %[xb] offset:1024\n"
    "global_load_dwordx4 v[36:39], %[vo], %[xb] offset:1152\n"
    "XL%=:\n"
    "s_load_dwordx2 s[66:67], %[wsb], 0x0\n"
    "s_load_dword s68, %[cp], 0x0\n"
    "s_load_dword s69, %[cp], %[c2]\n"
    :: [vo] "{v40}"((u32)(threadIdx.x >> 3) * 1280u + (threadIdx.x & 7) * 16u), "{v41}"(tid0), [xb] "s"(xg), [wsb] "s"(ws + W_NONCE), [cp] "s"(cp), [c2] "s"(c2), [wave] "s"(wave)
    : "v0", "v1", "v2", "v3", "v4", "v5", "v6", "v7", "v8", "v9", "v10", "v11", "v12", "v13", "v14", "v15", "v16", "v17", "v18", "v19", "v20", "v21", "v22", "v23", "v24", "v25", "v26", "v27", "v28", "v29", "v30", "v31", "v32", "v33", "v34", "v35", "v36", "v37", "v38", "v39", "v54", "s66", "s67", "s68", "s69", "scc", "memory");
  const int kw = q & 1;
  const u32 qsrc = 160u * c + 40u * q;
  const u32x4 srdq = mksrd(wq, 819200);
  char* ob = ws + W_O + (size_t)g * 81920 + c * 10240; char* ofl = ws + W_OF + g * 256 + (c * 4 + q) * 8;
  const char* kb = ws + W_KV + kw * 32 * KVT;
  const char* kt = ws + W_KV + (2 * g + (pp >> 1)) * KVT; const char* kf = ws + W_KVF + (g * 8 + c) * 8;
  u32x4 srdk = mksrd(ty ? wv : wk, 25600); u64 mval = ty ? 0x000F000F000F000Full : ~0ull, mst = ty ? 0x003F003F003F003Full : 0xFFFFull;
  asm volatile("" : "+s"(ob), "+s"(ofl), "+s"(kb), "+s"(kt), "+s"(kf), "+s"(srdk), "+s"(mval), "+s"(mst));
  if (wave < 4) {
    u64 epoch, sj; f32x32 xr0; f32x8 xr1; u32 jk;
    asm volatile("" : "={v[0:31]}"(xr0), "={v[32:39]}"(xr1), "={v54}"(jk));
    asm volatile("" : "={s[66:67]}"(epoch), "={s[68:69]}"(sj));
    LANE_VARS
    if (tid < 20) *(LAS u32*)(lds + L_FL + (tid < 16 ? tid : tid + 16) * 4) = 0u;
    LN_ADDR
    XB_ADDR
    const int rr = 8 * q + (lane >> 3), cg = lane & 7;
    const u32 tli = ((u32)jj * 4u + q) * 25u + (u32)(lane < 25 ? lane : 0);
    const u32 ob0 = (u32)((g * 64 + rr) * 640 + 160 * pp + 96 * ty + 4 * cg) * 2u;
    u32x4 a84 = {(u32)lane * 16u, 1024u + r * 8 + 1 * hi * KVT, 1280u + hi * 384 + (r < 21 ? r : 21) * 16, L0 + L_AR + 6144u * (q ^ 1) + lane * 16};
    u32x8 a92 = {L0 + L_RS + r * 4, L0 + L_FL + 48 + 4 * kw, L0 + L_AR + 6144u * q + lane * 16, L0 + L_FL + 16 + 4 * q,
                 L0 + L_FL + 16 + 4 * (q ^ 1), (u32)(q * 2048 + lane * 16), (u32)(8192 + hi * 1024 + ((q & 1) * 32 + r) * 16 + (q >> 1) * 8), 0u};
    u32x4 a110 = {ob0 + ty * 40960u, ob0 + (1 - ty) * 40960u, ob0 + ty * 40960u + (ty ? -64 : 128),
                  (u32)(lane >> 2) * 1280u + (u32)((lane & 3) < 3 ? (lane & 3) : 2) * 16u};
    u32x2 a122 = {L0 + L_AR + 6144u * q + hi * 512 + trl + (qsrc & 15u), L0 + q * PS + 20480u + hi * 512 + trl + (qsrc & 15u)};
    const u32x4 a104 = {(tli / 5u) * 1280u + (b & 1) * 640u + (tli % 5u) * 128u, 0u, 0u, 0u};
    asm volatile("" :: "{v[0:31]}"(xr0), "{v[32:39]}"(xr1), "{v54}"(jk), "{s[66:67]}"(epoch), "{s[68:69]}"(sj), "{v[84:87]}"(a84), "{v[92:99]}"(a92), "{v[100:103]}"(ln), "{v[104:107]}"(a104),
                       "{v[110:113]}"(a110), "{v[114:121]}"(xbv), "{v[122:123]}"(a122));
  } else {
    u64 epoch, sj;
    asm volatile("" : "={s[66:67]}"(epoch), "={s[68:69]}"(sj));
    LANE_VARS
    const int krow = 16 * pp + l15;
    LN_ADDR
    const int kr = 16 * (pp & 1) + l15;
    const u32 ka3 = L0 + L_RS + (ty ? 16 * pp + 4 * g4 : krow) * 4;
    const u32 ks0 = ty ? 1280 + (((pp & 1) * 2 + (g4 & 1)) * 24 + l15) * 16 + (g4 >> 1) * 8 : ((g4 & 1) * 32 + kr) * 16 + (g4 >> 1) * 8;
    u32x4 k84 = {(u32)lane * 16u, l15 == 4 ? 0x3F803F80u : 0u, (u32)(lane & 7) * 2048u, L0 + L_FL};
    u32x8 k92 = {ka3, ka3 + 4 * ty, ka3 + 8 * ty, ka3 + 12 * ty, L0 + 20480u + 6144u + lane * 16, L0 + 2 * PS + 20480u + 6144u + lane * 16, ks0, ty ? ks0 + 256 : 1024u + kr * 8};
    const int kch = 20 * (q & 1) + g4;
    const u32 kxa = L0 + (2 * (krow >> 5) + (q >> 1)) * PS + kch * 512;
    u32x4 k104 = {kxa + (((krow & 31) ^ (kch & 7)) << 4), kxa + (((krow & 31) ^ ((kch + 4) & 7)) << 4),
                  L0 + q * PS + 20480u + 6144u + (8 * g4 + (l15 >> 2)) * 40 + 8 * (lane & 3), L0 + q * PS + 20480u + 6144u + lane * 16};
    const u32 wi = (u32)jj * 4u + q, tl = wi * 25u + (u32)(lane < 25 ? lane : 0), kl = wi * 4u + (u32)(lane - 25);
    u64 tp = (u64)wq + (tl / 5u) * 1280u + (b & 1) * 640u + (tl % 5u) * 128u;
    if (lane >= 25 && lane < 29 && kl < 400u) tp = (kl < 200u ? (u64)wk : (u64)wv - 25600u) + kl * 128u;
    asm volatile(
    "global_load_dwordx4 v[0:3], %[vo], %[xb] offset:0\n"
    "global_load_dwordx4 v[4:7], %[vo], %[xb] offset:128\n"
    "global_load_dwordx4 v[8:11], %[vo], %[xb] offset:256\n"
    "global_load_dwordx4 v[12:15], %[vo], %[xb] offset:384\n"
    "global_load_dwordx4 v[16:19], %[vo], %[xb] offset:512\n"
    "global_load_dwordx4 v[20:23], %[vo], %[xb] offset:640\n"
    "global_load_dwordx4 v[24:27], %[vo], %[xb] offset:768\n"
    "global_load_dwordx4 v[28:31], %[vo], %[xb] offset:896\n"
    "global_load_dwordx4 v[32:35], %[vo], %[xb] offset:1024\n"
    "global_load_dwordx4 v[36:39], %[vo], %[xb] offset:1152\n"
      :: "{v[108:109]}"(tp), "{s[66:67]}"(epoch), "{s[68:69]}"(sj), "{v[84:87]}"(k84), "{v[92:99]}"(k92), "{v[100:103]}"(ln), "{v[104:107]}"(k104), [vo] "{v40}"(vo), [xb] "s"(xg) : "v0", "v1", "v2", "v3", "v4", "v5", "v6", "v7", "v8", "v9", "v10", "v11", "v12", "v13", "v14", "v15", "v16", "v17", "v18", "v19", "v20", "v21", "v22", "v23", "v24", "v25", "v26", "v27", "v28", "v29", "v30", "v31", "v32", "v33", "v34", "v35", "v36", "v37", "v38", "v39", "v54", "memory");
  }
  {
    f32x32 xr0; f32x8 xr1; u64 epoch, sj; u32x4 p84, p100, p104, p110; u32x8 p92, p114; u32x2 p122; u32 junk0;
    asm volatile("" : "={v[0:31]}"(xr0), "={v[32:39]}"(xr1), "={v[84:87]}"(p84), "={v[92:99]}"(p92), "={v[100:103]}"(p100), "={v[104:107]}"(p104),
                      "={v[110:113]}"(p110), "={v[114:121]}"(p114), "={v[122:123]}"(p122), "={v54}"(junk0));
    asm volatile("" : "={s[66:67]}"(epoch), "={s[68:69]}"(sj));
    asm volatile(
    "s_mov_b32 s60, 0x3f803f80\n"
    "v_mov_b32 v42, 0\n"
    "v_mov_b32 v43, 0\n"
    "v_mov_b32 v44, 0\n"
    "v_mov_b32 v45, 0\n"
    "s_waitcnt vmcnt(9)\n"
    "v_dot2c_f32_bf16 v42, s60, v0\n"
    "v_dot2c_f32_bf16 v44, v0, v0\n"
    "v_dot2c_f32_bf16 v43, s60, v1\n"
    "v_dot2c_f32_bf16 v45, v1, v1\n"
    "v_dot2c_f32_bf16 v42, s60, v2\n"
    "v_dot2c_f32_bf16 v44, v2, v2\n"
    "v_dot2c_f32_bf16 v43, s60, v3\n"
    "v_dot2c_f32_bf16 v45, v3, v3\n"
    "s_waitcnt vmcnt(8)\n"
    "v_dot2c_f32_bf16 v42, s60, v4\n"
    "v_dot2c_f32_bf16 v44, v4, v4\n"
    "v_dot2c_f32_bf16 v43, s60, v5\n"
    "v_dot2c_f32_bf16 v45, v5, v5\n"
    "v_dot2c_f32_bf16 v42, s60, v6\n"
    "v_dot2c_f32_bf16 v44, v6, v6\n"
    "v_dot2c_f32_bf16 v43, s60, v7\n"
    "v_dot2c_f32_bf16 v45, v7, v7\n"
    "s_waitcnt vmcnt(7)\n"
    "v_dot2c_f32_bf16 v42, s60, v8\n"
    "v_dot2c_f32_bf16 v44, v8, v8\n"
    "v_dot2c_f32_bf16 v43, s60, v9\n"
    "v_dot2c_f32_bf16 v45, v9, v9\n"
    "v_dot2c_f32_bf16 v42, s60, v10\n"
    "v_dot2c_f32_bf16 v44, v10, v10\n"
    "v_dot2c_f32_bf16 v43, s60, v11\n"
    "v_dot2c_f32_bf16 v45, v11, v11\n"
    "s_waitcnt vmcnt(6)\n"
    "v_dot2c_f32_bf16 v42, s60, v12\n"
    "v_dot2c_f32_bf16 v44, v12, v12\n"
    "v_dot2c_f32_bf16 v43, s60, v13\n"
    "v_dot2c_f32_bf16 v45, v13, v13\n"
    "v_dot2c_f32_bf16 v42, s60, v14\n"
    "v_dot2c_f32_bf16 v44, v14, v14\n"
    "v_dot2c_f32_bf16 v43, s60, v15\n"
    "v_dot2c_f32_bf16 v45, v15, v15\n"
    "s_waitcnt vmcnt(5)\n"
    "v_dot2c_f32_bf16 v42, s60, v16\n"
    "v_dot2c_f32_bf16 v44, v16, v16\n"
    "v_dot2c_f32_bf16 v43, s60, v17\n"
    "v_dot2c_f32_bf16 v45, v17, v17\n"
    "v_dot2c_f32_bf16 v42, s60, v18\n"
    "v_dot2c_f32_bf16 v44, v18, v18\n"
    "v_dot2c_f32_bf16 v43, s60, v19\n"
    "v_dot2c_f32_bf16 v45, v19, v19\n"
    "s_waitcnt vmcnt(4)\n"
    "v_dot2c_f32_bf16 v42, s60, v20\n"
    "v_dot2c_f32_bf16 v44, v20, v20\n"
    "v_dot2c_f32_bf16 v43, s60, v21\n"
    "v_dot2c_f32_bf16 v45, v21, v21\n"
    "v_dot2c_f32_bf16 v42, s60, v22\n"
    "v_dot2c_f32_bf16 v44, v22, v22\n"
    "v_dot2c_f32_bf16 v43, s60, v23\n"
    "v_dot2c_f32_bf16 v45, v23, v23\n"
    "s_waitcnt vmcnt(3)\n"
    "v_dot2c_f32_bf16 v42, s60, v24\n"
    "v_dot2c_f32_bf16 v44, v24, v24\n"
    "v_dot2c_f32_bf16 v43, s60, v25\n"
    "v_dot2c_f32_bf16 v45, v25, v25\n"
    "v_dot2c_f32_bf16 v42, s60, v26\n"
    "v_dot2c_f32_bf16 v44, v26, v26\n"
    "v_dot2c_f32_bf16 v43, s60, v27\n"
    "v_dot2c_f32_bf16 v45, v27, v27\n"
    "s_waitcnt vmcnt(2)\n"
    "v_dot2c_f32_bf16 v42, s60, v28\n"
    "v_dot2c_f32_bf16 v44, v28, v28\n"
    "v_dot2c_f32_bf16 v43, s60, v29\n"
    "v_dot2c_f32_bf16 v45, v29, v29\n"
    "v_dot2c_f32_bf16 v42, s60, v30\n"
    "v_dot2c_f32_bf16 v44, v30, v30\n"
    "v_dot2c_f32_bf16 v43, s60, v31\n"
    "v_dot2c_f32_bf16 v45, v31, v31\n"
    "s_waitcnt vmcnt(1)\n"
    "v_dot2c_f32_bf16 v42, s60, v32\n"
    "v_dot2c_f32_bf16 v44, v32, v32\n"
    "v_dot2c_f32_bf16 v43, s60, v33\n"
    "v_dot2c_f32_bf16 v45, v33, v33\n"
    "v_dot2c_f32_bf16 v42, s60, v34\n"
    "v_dot2c_f32_bf16 v44, v34, v34\n"
    "v_dot2c_f32_bf16 v43, s60, v35\n"
    "v_dot2c_f32_bf16 v45, v35, v35\n"
    "s_waitcnt vmcnt(0)\n"
    "v_dot2c_f32_bf16 v42, s60, v36\n"
    "v_dot2c_f32_bf16 v44, v36, v36\n"
    "v_dot2c_f32_bf16 v43, s60, v37\n"
    "v_dot2c_f32_bf16 v45, v37, v37\n"
    "v_dot2c_f32_bf16 v42, s60, v38\n"
    "v_dot2c_f32_bf16 v44, v38, v38\n"
    "v_dot2c_f32_bf16 v43, s60, v39\n"
    "v_dot2c_f32_bf16 v45, v39, v39\n"
    "s_mov_b32 s61, 0x00003f80\n"
    "s_mov_b32 s62, 0x3f800000\n"
    "s_mov_b32 s65, m0\n"
    "s_nop 0\n"
    "v_add_f32 v42, v42, v43\n"
    "v_add_f32 v44, v44, v45\n"
    "s_nop 0\n"
    "v_add_f32_dpp v42, v42, v42 quad_perm:[1,0,3,2] row_mask:0xf bank_mask:0xf\n"
    "v_add_f32_dpp v44, v44, v44 quad_perm:[1,0,3,2] row_mask:0xf bank_mask:0xf\n"
    "s_nop 0\n"
    "v_add_f32_dpp v42, v42, v42 quad_perm:[2,3,0,1] row_mask:0xf bank_mask:0xf\n"
    "v_add_f32_dpp v44, v44, v44 quad_perm:[2,3,0,1] row_mask:0xf bank_mask:0xf\n"
    "s_nop 0\n"
    "v_add_f32_dpp v42, v42, v42 row_half_mirror row_mask:0xf bank_mask:0xf\n"
    "v_add_f32_dpp v44, v44, v44 row_half_mirror row_mask:0xf bank_mask:0xf\n"
    "s_nop 0\n"
    "v_mul_f32 v42, 0xbacccccd, v42\n"
    "v_mul_f32 v44, 0x3acccccd, v44\n"
    "v_fma_f32 v44, -v42, v42, v44\n"
    "v_max_f32 v44, 0, v44\n"
    "v_add_f32 v44, 0x3727c5ac, v44\n"
    "v_rsq_f32 v44, v44\n"
    "v_dot2_f32_bf16 v46, v0, s61, v42\n"
    "v_dot2_f32_bf16 v47, v0, s62, v42\n"
    "v_dot2_f32_bf16 v48, v1, s61, v42\n"
    "v_dot2_f32_bf16 v49, v1, s62, v42\n"
    "ds_write_b32 v101, v44\n"
    "v_dot2_f32_bf16 v50, v2, s61, v42\n"
    "v_dot2_f32_bf16 v51, v2, s62, v42\n"
    "v_cvt_pk_bf16_f32 v0, v46, v47\n"
    "v_dot2_f32_bf16 v52, v3, s61, v42\n"
    "v_dot2_f32_bf16 v53, v3, s62, v42\n"
    "v_cvt_pk_bf16_f32 v1, v48, v49\n"
    "v_dot2_f32_bf16 v46, v4, s61, v42\n"
    "v_dot2_f32_bf16 v47, v4, s62, v42\n"
    "v_cvt_pk_bf16_f32 v2, v50, v51\n"
    "v_dot2_f32_bf16 v48, v5, s61, v42\n"
    "v_dot2_f32_bf16 v49, v5, s62, v42\n"
    "v_cvt_pk_bf16_f32 v3, v52, v53\n"
    "ds_write_b128 v100, v[0:3] offset:0\n"
    "v_dot2_f32_bf16 v50, v6, s61, v42\n"
    "v_dot2_f32_bf16 v51, v6, s62, v42\n"
    "v_cvt_pk_bf16_f32 v4, v46, v47\n"
    "v_dot2_f32_bf16 v52, v7, s61, v42\n"
    "v_dot2_f32_bf16 v53, v7, s62, v42\n"
    "v_cvt_pk_bf16_f32 v5, v48, v49\n"
    "v_dot2_f32_bf16 v46, v8, s61, v42\n"
    "v_dot2_f32_bf16 v47, v8, s62, v42\n"
    "v_cvt_pk_bf16_f32 v6, v50, v51\n"
    "v_dot2_f32_bf16 v48, v9, s61, v42\n"
    "v_dot2_f32_bf16 v49, v9, s62, v42\n"
    "v_cvt_pk_bf16_f32 v7, v52, v53\n"
    "ds_write_b128 v100, v[4:7] offset:4096\n"
    "v_dot2_f32_bf16 v50, v10, s61, v42\n"
    "v_dot2_f32_bf16 v51, v10, s62, v42\n"
    "v_cvt_pk_bf16_f32 v8, v46, v47\n"
    "v_dot2_f32_bf16 v52, v11, s61, v42\n"
    "v_dot2_f32_bf16 v53, v11, s62, v42\n"
    "v_cvt_pk_bf16_f32 v9, v48, v49\n"
    "v_dot2_f32_bf16 v46, v12, s61, v42\n"
    "v_dot2_f32_bf16 v47, v12, s62, v42\n"
    "v_cvt_pk_bf16_f32 v10, v50, v51\n"
    "v_dot2_f32_bf16 v48, v13, s61, v42\n"
    "v_dot2_f32_bf16 v49, v13, s62, v42\n"
    "v_cvt_pk_bf16_f32 v11, v52, v53\n"
    "ds_write_b128 v100, v[8:11] offset:8192\n"
    "v_dot2_f32_bf16 v50, v14, s61, v42\n"
    "v_dot2_f32_bf16 v51, v14, s62, v42\n"
    "v_cvt_pk_bf16_f32 v12, v46, v47\n"
    "v_dot2_f32_bf16 v52, v15, s61, v42\n"
    "v_dot2_f32_bf16 v53, v15, s62, v42\n"
    "v_cvt_pk_bf16_f32 v13, v48, v49\n"
    "v_dot2_f32_bf16 v46, v16, s61, v42\n"
    "v_dot2_f32_bf16 v47, v16, s62, v42\n"
    "v_cvt_pk_bf16_f32 v14, v50, v51\n"
    "s_cmp_gt_u32 %[wave], 3\n"
    "s_cbranch_scc1 MA%=\n"
    "s_add_u32 m0, %[m0k], 0\n"
    "s_add_u32 s64, %[sofk], 0\n"
    "buffer_load_dwordx4 v84, %[srdk], s64 offen lds\n"
    "s_add_u32 m0, %[m0k], 1024\n"
    "s_add_u32 s64, %[sofk], 1024\n"
    "buffer_load_dwordx4 v84, %[srdk], s64 offen lds\n"
    "s_add_u32 m0, %[m0k], 2048\n"
    "s_add_u32 s64, %[sofk], 2048\n"
    "buffer_load_dwordx4 v84, %[srdk], s64 offen lds\n"
    "MA%=:\n"
    "v_dot2_f32_bf16 v48, v17, s61, v42\n"
    "v_dot2_f32_bf16 v49, v17, s62, v42\n"
    "v_cvt_pk_bf16_f32 v15, v52, v53\n"
    "ds_write_b128 v100, v[12:15] offset:12288\n"
    "v_dot2_f32_bf16 v50, v18, s61, v42\n"
    "v_dot2_f32_bf16 v51, v18, s62, v42\n"
    "v_cvt_pk_bf16_f32 v16, v46, v47\n"
    "s_nop 0\n"
    "v_dot2_f32_bf16 v52, v19, s61, v42\n"
    "v_dot2_f32_bf16 v53, v19, s62, v42\n"
    "v_cvt_pk_bf16_f32 v17, v48, v49\n"
    "s_nop 0\n"
    "v_dot2_f32_bf16 v46, v20, s61, v42\n"
    "v_dot2_f32_bf16 v47, v20, s62, v42\n"
    "v_cvt_pk_bf16_f32 v18, v50, v51\n"
    "s_cmp_gt_u32 %[wave], 3\n"
    "s_cbranch_scc1 MB%=\n"
    "s_add_u32 m0, %[m0k], 3072\n"
    "s_add_u32 s64, %[sofk], 3072\n"
    "buffer_load_dwordx4 v84, %[srdk], s64 offen lds\n"
    "s_add_u32 m0, %[m0k], 4096\n"
    "s_add_u32 s64, %[sofk], 4096\n"
    "buffer_load_dwordx4 v84, %[srdk], s64 offen lds\n"
    "s_add_u32 m0, %[m0k], 5120\n"
    "s_add_u32 s64, %[sofk], 5120\n"
    "buffer_load_dwordx4 v84, %[srdk], s64 offen lds\n"
    "MB%=:\n"
    "v_dot2_f32_bf16 v48, v21, s61, v42\n"
    "v_dot2_f32_bf16 v49, v21, s62, v42\n"
    "v_cvt_pk_bf16_f32 v19, v52, v53\n"
    "ds_write_b128 v100, v[16:19] offset:16384\n"
    "v_dot2_f32_bf16 v50, v22, s61, v42\n"
    "v_dot2_f32_bf16 v51, v22, s62, v42\n"
    "v_cvt_pk_bf16_f32 v20, v46, v47\n"
    "s_nop 0\n"
    "v_dot2_f32_bf16 v52, v23, s61, v42\n"
    "v_dot2_f32_bf16 v53, v23, s62, v42\n"
    "v_cvt_pk_bf16_f32 v21, v48, v49\n"
    "s_nop 0\n"
    "v_dot2_f32_bf16 v46, v24, s61, v42\n"
    "v_dot2_f32_bf16 v47, v24, s62, v42\n"
    "v_cvt_pk_bf16_f32 v22, v50, v51\n"
    "s_cmp_gt_u32 %[wave], 3\n"
    "s_cbranch_scc1 MC%=\n"
    "s_mov_b64 exec, 0xffff\n"
    "s_add_u32 m0, %[m0k], 6144\n"
    "s_add_u32 s64, %[sofk], 6144\n"
    "buffer_load_dwordx4 v84, %[srdk], s64 offen lds\n"
    "s_mov_b64 exec, -1\n"
    "global_load_dword v54, v104, %[wqp]\n"
    "MC%=:\n"
    "v_dot2_f32_bf16 v48, v25, s61, v42\n"
    "v_dot2_f32_bf16 v49, v25, s62, v42\n"
    "v_cvt_pk_bf16_f32 v23, v52, v53\n"
    "ds_write_b128 v100, v[20:23] offset:34752\n"
    "v_dot2_f32_bf16 v50, v26, s61, v42\n"
    "v_dot2_f32_bf16 v51, v26, s62, v42\n"
    "v_cvt_pk_bf16_f32 v24, v46, v47\n"
    "v_dot2_f32_bf16 v52, v27, s61, v42\n"
    "v_dot2_f32_bf16 v53, v27, s62, v42\n"
    "v_cvt_pk_bf16_f32 v25, v48, v49\n"
    "v_dot2_f32_bf16 v46, v28, s61, v42\n"
    "v_dot2_f32_bf16 v47, v28, s62, v42\n"
    "v_cvt_pk_bf16_f32 v26, v50, v51\n"
    "v_dot2_f32_bf16 v48, v29, s61, v42\n"
    "v_dot2_f32_bf16 v49, v29, s62, v42\n"
    "v_cvt_pk_bf16_f32 v27, v52, v53\n"
    "ds_write_b128 v100, v[24:27] offset:38848\n"
    "v_dot2_f32_bf16 v50, v30, s61, v42\n"
    "v_dot2_f32_bf16 v51, v30, s62, v42\n"
    "v_cvt_pk_bf16_f32 v28, v46, v47\n"
    "v_dot2_f32_bf16 v52, v31, s61, v42\n"
    "v_dot2_f32_bf16 v53, v31, s62, v42\n"
    "v_cvt_pk_bf16_f32 v29, v48, v49\n"
    "v_dot2_f32_bf16 v46, v32, s61, v42\n"
    "v_dot2_f32_bf16 v47, v32, s62, v42\n"
    "v_cvt_pk_bf16_f32 v30, v50, v51\n"
    "v_dot2_f32_bf16 v48, v33, s61, v42\n"
    "v_dot2_f32_bf16 v49, v33, s62, v42\n"
    "v_cvt_pk_bf16_f32 v31, v52, v53\n"
    "ds_write_b128 v100, v[28:31] offset:42944\n"
    "v_dot2_f32_bf16 v50, v34, s61, v42\n"
    "v_dot2_f32_bf16 v51, v34, s62, v42\n"
    "v_cvt_pk_bf16_f32 v32, v46, v47\n"
    "v_dot2_f32_bf16 v52, v35, s61, v42\n"
    "v_dot2_f32_bf16 v53, v35, s62, v42\n"
    "v_cvt_pk_bf16_f32 v33, v48, v49\n"
    "v_dot2_f32_bf16 v46, v36, s61, v42\n"
    "v_dot2_f32_bf16 v47, v36, s62, v42\n"
    "v_cvt_pk_bf16_f32 v34, v50, v51\n"
    "v_dot2_f32_bf16 v48, v37, s61, v42\n"
    "v_dot2_f32_bf16 v49, v37, s62, v42\n"
    "v_cvt_pk_bf16_f32 v35, v52, v53\n"
    "ds_write_b128 v100, v[32:35] offset:47040\n"
    "v_dot2_f32_bf16 v50, v38, s61, v42\n"
    "v_dot2_f32_bf16 v51, v38, s62, v42\n"
    "v_cvt_pk_bf16_f32 v36, v46, v47\n"
    "v_dot2_f32_bf16 v52, v39, s61, v42\n"
    "v_dot2_f32_bf16 v53, v39, s62, v42\n"
    "v_cvt_pk_bf16_f32 v37, v48, v49\n"
    "s_nop 1\n"
    "v_cvt_pk_bf16_f32 v38, v50, v51\n"
    "s_nop 1\n"
    "v_cvt_pk_bf16_f32 v39, v52, v53\n"
    "ds_write_b128 v100, v[36:39] offset:51136\n"
    "s_waitcnt lgkmcnt(0)\n"
    "s_cmp_gt_u32 %[wave], 3\n"
    "s_cbranch_scc1 MW%=\n"
    "s_waitcnt vmcnt(1)\n"
    "MW%=:\n"
    "s_add_u32 s66, s66, 1\n"
    "s_addc_u32 s67, s67, 0\n"
    "v_mov_b32 v40, s66\n"
    "v_mov_b32 v41, s67\n"
    "ds_write_b64 v102, v[40:41]\n"
    "s_cmp_gt_u32 %[wave], 3\n"
    "s_cbranch_scc1 MR%=\n"
    "s_add_u32 m0, %[m0b], 0\n"
    "s_add_u32 s64, %[soff], 0\n"
    "buffer_load_dwordx4 v113, %[srd], s64 offen lds\n"
    "s_add_u32 m0, %[m0b], 1024\n"
    "s_add_u32 s64, %[soff], 20480\n"
    "buffer_load_dwordx4 v113, %[srd], s64 offen lds\n"
    "s_add_u32 m0, %[m0b], 2048\n"
    "s_add_u32 s64, %[soff], 40960\n"
    "buffer_load_dwordx4 v113, %[srd], s64 offen lds\n"
    "s_add_u32 m0, %[m0b], 3072\n"
    "s_add_u32 s64, %[soff], 61440\n"
    "buffer_load_dwordx4 v113, %[srd], s64 offen lds\n"
    "s_add_u32 m0, %[m0b], 4096\n"
    "s_add_u32 s64, %[soff], 81920\n"
    "buffer_load_dwordx4 v113, %[srd], s64 offen lds\n"
    "s_add_u32 m0, %[m0b], 5120\n"
    "s_add_u32 s64, %[soff], 102400\n"
    "buffer_load_dwordx4 v113, %[srd], s64 offen lds\n"
    "s_add_u32 m0, %[m0c], 0\n"
    "s_add_u32 s64, %[soff], 122880\n"
    "buffer_load_dwordx4 v113, %[srd], s64 offen lds\n"
    "s_add_u32 m0, %[m0c], 1024\n"
    "s_add_u32 s64, %[soff], 143360\n"
    "buffer_load_dwordx4 v113, %[srd], s64 offen lds\n"
    "s_add_u32 m0, %[m0c], 2048\n"
    "s_add_u32 s64, %[soff], 163840\n"
    "buffer_load_dwordx4 v113, %[srd], s64 offen lds\n"
    "s_add_u32 m0, %[m0c], 3072\n"
    "s_add_u32 s64, %[soff], 184320\n"
    "buffer_load_dwordx4 v113, %[srd], s64 offen lds\n"
    "s_add_u32 m0, %[m0c], 4096\n"
    "s_add_u32 s64, %[soff], 204800\n"
    "buffer_load_dwordx4 v113, %[srd], s64 offen lds\n"
    "s_add_u32 m0, %[m0c], 5120\n"
    "s_add_u32 s64, %[soff], 225280\n"
    "buffer_load_dwordx4 v113, %[srd], s64 offen lds\n"
    "v_accvgpr_write_b32 a0, 0\n"
    "v_accvgpr_write_b32 a1, 0\n"
    "v_accvgpr_write_b32 a2, 0\n"
    "v_accvgpr_write_b32 a3, 0\n"
    "v_accvgpr_write_b32 a4, 0\n"
    "v_accvgpr_write_b32 a5, 0\n"
    "v_accvgpr_write_b32 a6, 0\n"
    "v_accvgpr_write_b32 a7, 0\n"
    "v_accvgpr_write_b32 a8, 0\n"
    "v_accvgpr_write_b32 a9, 0\n"
    "v_accvgpr_write_b32 a10, 0\n"
    "v_accvgpr_write_b32 a11, 0\n"
    "v_accvgpr_write_b32 a12, 0\n"
    "v_accvgpr_write_b32 a13, 0\n"
    "v_accvgpr_write_b32 a14, 0\n"
    "v_accvgpr_write_b32 a15, 0\n"
    "v_accvgpr_write_b32 a16, 0\n"
    "v_accvgpr_write_b32 a17, 0\n"
    "v_accvgpr_write_b32 a18, 0\n"
    "v_accvgpr_write_b32 a19, 0\n"
    "v_accvgpr_write_b32 a20, 0\n"
    "v_accvgpr_write_b32 a21, 0\n"
    "v_accvgpr_write_b32 a22, 0\n"
    "v_accvgpr_write_b32 a23, 0\n"
    "v_accvgpr_write_b32 a24, 0\n"
    "v_accvgpr_write_b32 a25, 0\n"
    "v_accvgpr_write_b32 a26, 0\n"
    "v_accvgpr_write_b32 a27, 0\n"
    "v_accvgpr_write_b32 a28, 0\n"
    "v_accvgpr_write_b32 a29, 0\n"
    "v_accvgpr_write_b32 a30, 0\n"
    "v_accvgpr_write_b32 a31, 0\n"
    "v_accvgpr_write_b32 a32, 0\n"
    "v_accvgpr_write_b32 a33, 0\n"
    "v_accvgpr_write_b32 a34, 0\n"
    "v_accvgpr_write_b32 a35, 0\n"
    "v_accvgpr_write_b32 a36, 0\n"
    "v_accvgpr_write_b32 a37, 0\n"
    "v_accvgpr_write_b32 a38, 0\n"
    "v_accvgpr_write_b32 a39, 0\n"
    "v_accvgpr_write_b32 a40, 0\n"
    "v_accvgpr_write_b32 a41, 0\n"
    "v_accvgpr_write_b32 a42, 0\n"
    "v_accvgpr_write_b32 a43, 0\n"
    "v_accvgpr_write_b32 a44, 0\n"
    "v_accvgpr_write_b32 a45, 0\n"
    "v_accvgpr_write_b32 a46, 0\n"
    "v_accvgpr_write_b32 a47, 0\n"
    "v_accvgpr_write_b32 a48, 0\n"
    "v_accvgpr_write_b32 a49, 0\n"
    "v_accvgpr_write_b32 a50, 0\n"
    "v_accvgpr_write_b32 a51, 0\n"
    "v_accvgpr_write_b32 a52, 0\n"
    "v_accvgpr_write_b32 a53, 0\n"
    "v_accvgpr_write_b32 a54, 0\n"
    "v_accvgpr_write_b32 a55, 0\n"
    "v_accvgpr_write_b32 a56, 0\n"
    "v_accvgpr_write_b32 a57, 0\n"
    "v_accvgpr_write_b32 a58, 0\n"
    "v_accvgpr_write_b32 a59, 0\n"
    "v_accvgpr_write_b32 a60, 0\n"
    "v_accvgpr_write_b32 a61, 0\n"
    "v_accvgpr_write_b32 a62, 0\n"
    "v_accvgpr_write_b32 a63, 0\n"
    "v_accvgpr_write_b32 a116, 0\n"
    "v_accvgpr_write_b32 a117, 0\n"
    "v_accvgpr_write_b32 a118, 0\n"
    "v_accvgpr_write_b32 a119, 0\n"
    "MR%=:\n"
    "s_mov_b32 s62, 0x100000\n"
    "LBS%=:\n"
    "ds_read_b64 v[46:47], v103\n"
    "s_sub_u32 s62, s62, 1\n"
    "s_cmp_eq_u32 s62, 0\n"
    "s_cbranch_scc1 LBX%=\n"
    "s_waitcnt lgkmcnt(0)\n"
    "v_cmp_eq_u64 vcc, v[46:47], v[40:41]\n"
    "s_nop 1\n"
    "s_cmp_lg_u64 vcc, exec\n"
    "s_cbranch_scc1 LBS%=\n"
    "LBX%=:\n"
    "s_waitcnt lgkmcnt(0)\n"
    "s_mov_b32 m0, s65\n"
    "s_cmp_lt_u32 %[wave], 4\n"
    "s_cbranch_scc1 LKA%=\n"
    "s_setprio 3\n"
    "s_waitcnt vmcnt(0)\n"
    "ds_read_b128 v[0:3], v104 offset:0\n"
    "ds_read_b64_tr_b16 v[20:21], v106 offset:0\n"
    "ds_read_b64_tr_b16 v[22:23], v106 offset:160\n"
    "ds_read_b64_tr_b16 v[40:41], v106 offset:32\n"
    "ds_read_b64_tr_b16 v[42:43], v106 offset:192\n"
    "ds_read_b128 v[4:7], v105 offset:2048\n"
    "ds_read_b64_tr_b16 v[24:25], v106 offset:1280\n"
    "ds_read_b64_tr_b16 v[26:27], v106 offset:1440\n"
    "ds_read_b64_tr_b16 v[44:45], v106 offset:1312\n"
    "ds_read_b64_tr_b16 v[46:47], v106 offset:1472\n"
    "ds_read_b128 v[8:11], v104 offset:4096\n"
    "ds_read_b64_tr_b16 v[28:29], v106 offset:2560\n"
    "ds_read_b64_tr_b16 v[30:31], v106 offset:2720\n"
    "ds_read_b64_tr_b16 v[48:49], v106 offset:2592\n"
    "ds_read_b64_tr_b16 v[50:51], v106 offset:2752\n"
    "ds_read_b128 v[12:15], v105 offset:6144\n"
    "ds_read_b64_tr_b16 v[32:33], v106 offset:3840\n"
    "ds_read_b64_tr_b16 v[34:35], v106 offset:4000\n"
    "ds_read_b64_tr_b16 v[52:53], v106 offset:3872\n"
    "ds_read_b64_tr_b16 v[54:55], v106 offset:4032\n"
    "ds_read_b128 v[16:19], v104 offset:8192\n"
    "ds_read_b64_tr_b16 v[36:37], v106 offset:5120\n"
    "ds_read_b64_tr_b16 v[38:39], v106 offset:5280\n"
    "ds_read_b64_tr_b16 v[56:57], v106 offset:5152\n"
    "ds_read_b64_tr_b16 v[58:59], v106 offset:5312\n"
    "v_mov_b32 v68, 1\n"
    "v_mov_b64 v[70:71], s[66:67]\n"
    "s_cmp_lg_u32 %[ty], 0\n"
    "s_cbranch_scc1 KVV%=\n"
    "s_waitcnt lgkmcnt(15)\n"
    "v_mfma_f32_16x16x32_bf16 v[60:63], v[20:23], v[0:3], 0\n"
    "v_mfma_f32_16x16x32_bf16 v[64:67], v[40:43], v[0:3], 0\n"
    "s_waitcnt lgkmcnt(15)\n"
    "v_mfma_f32_16x16x32_bf16 v[60:63], v[24:27], v[4:7], v[60:63]\n"
    "v_mfma_f32_16x16x32_bf16 v[64:67], v[44:47], v[4:7], v[64:67]\n"
    "s_waitcnt lgkmcnt(10)\n"
    "v_mfma_f32_16x16x32_bf16 v[60:63], v[28:31], v[8:11], v[60:63]\n"
    "v_mfma_f32_16x16x32_bf16 v[64:67], v[48:51], v[8:11], v[64:67]\n"
    "s_waitcnt lgkmcnt(5)\n"
    "v_mfma_f32_16x16x32_bf16 v[60:63], v[32:35], v[12:15], v[60:63]\n"
    "v_mfma_f32_16x16x32_bf16 v[64:67], v[52:55], v[12:15], v[64:67]\n"
    "s_waitcnt lgkmcnt(0)\n"
    "v_mfma_f32_16x16x32_bf16 v[60:63], v[36:39], v[16:19], v[60:63]\n"
    "v_mfma_f32_16x16x32_bf16 v[64:67], v[56:59], v[16:19], v[64:67]\n"
    "s_branch KVW%=\n"
    "KVV%=:\n"
    "s_waitcnt lgkmcnt(15)\n"
    "v_mfma_f32_16x16x32_bf16 v[60:63], v[0:3], v[20:23], 0\n"
    "v_mfma_f32_16x16x32_bf16 v[64:67], v[0:3], v[40:43], 0\n"
    "s_waitcnt lgkmcnt(15)\n"
    "v_mfma_f32_16x16x32_bf16 v[60:63], v[4:7], v[24:27], v[60:63]\n"
    "v_mfma_f32_16x16x32_bf16 v[64:67], v[4:7], v[44:47], v[64:67]\n"
    "s_waitcnt lgkmcnt(10)\n"
    "v_mfma_f32_16x16x32_bf16 v[60:63], v[8:11], v[28:31], v[60:63]\n"
    "v_mfma_f32_16x16x32_bf16 v[64:67], v[8:11], v[48:51], v[64:67]\n"
    "s_waitcnt lgkmcnt(5)\n"
    "v_mfma_f32_16x16x32_bf16 v[60:63], v[12:15], v[32:35], v[60:63]\n"
    "v_mfma_f32_16x16x32_bf16 v[64:67], v[12:15], v[52:55], v[64:67]\n"
    "s_waitcnt lgkmcnt(0)\n"
    "v_mfma_f32_16x16x32_bf16 v[60:63], v[16:19], v[36:39], v[60:63]\n"
    "v_mfma_f32_16x16x32_bf16 v[64:67], v[16:19], v[56:59], v[64:67]\n"
    "KVW%=:\n"
    "s_cmp_eq_u32 %[q], 3\n"
    "s_cbranch_scc1 KVF%=\n"
    "s_nop 5\n"
    "ds_write_b128 v107, v[60:63] offset:6144\n"
    "ds_write_b128 v107, v[64:67]\n"
    "s_waitcnt lgkmcnt(0)\n"
    "s_mov_b64 exec, 1\n"
    "ds_add_u32 v87, v68\n"
    "s_mov_b64 exec, -1\n"
    "s_branch KVE%=\n"
    "KVF%=:\n"
    "ds_read_b32 v76, v92\n"
    "ds_read_b32 v77, v93\n"
    "ds_read_b32 v78, v94\n"
    "ds_read_b32 v79, v95\n"
    "s_mov_b32 s62, 0x100000\n"
    "KSS%=:\n"
    "ds_read_b32 v1, v87 offset:0\n"
    "s_sub_u32 s62, s62, 1\n"
    "s_cmp_eq_u32 s62, 0\n"
    "s_cbranch_scc1 KSX%=\n"
    "s_waitcnt lgkmcnt(0)\n"
    "v_readfirstlane_b32 s63, v1\n"
    "s_nop 0\n"
    "s_cmp_lt_u32 s63, 3\n"
    "s_cbranch_scc1 KSS%=\n"
    "KSX%=:\n"
    "s_waitcnt lgkmcnt(0)\n"
    "ds_read_b128 v[4:7], v96 offset:6144\n"
    "ds_read_b128 v[8:11], v96 offset:0\n"
    "ds_read_b128 v[12:15], v96 offset:40896\n"
    "ds_read_b128 v[16:19], v96 offset:34752\n"
    "ds_read_b128 v[20:23], v97 offset:6144\n"
    "ds_read_b128 v[24:27], v97 offset:0\n"
    "s_waitcnt lgkmcnt(4)\n"
    "v_add_f32 v60, v60, v4\n"
    "v_add_f32 v61, v61, v5\n"
    "v_add_f32 v62, v62, v6\n"
    "v_add_f32 v63, v63, v7\n"
    "v_add_f32 v64, v64, v8\n"
    "v_add_f32 v65, v65, v9\n"
    "v_add_f32 v66, v66, v10\n"
    "v_add_f32 v67, v67, v11\n"
    "s_waitcnt lgkmcnt(2)\n"
    "v_add_f32 v60, v60, v12\n"
    "v_add_f32 v61, v61, v13\n"
    "v_add_f32 v62, v62, v14\n"
    "v_add_f32 v63, v63, v15\n"
    "v_add_f32 v64, v64, v16\n"
    "v_add_f32 v65, v65, v17\n"
    "v_add_f32 v66, v66, v18\n"
    "v_add_f32 v67, v67, v19\n"
    "s_waitcnt lgkmcnt(0)\n"
    "v_add_f32 v60, v60, v20\n"
    "v_add_f32 v61, v61, v21\n"
    "v_add_f32 v62, v62, v22\n"
    "v_add_f32 v63, v63, v23\n"
    "v_add_f32 v64, v64, v24\n"
    "v_add_f32 v65, v65, v25\n"
    "v_add_f32 v66, v66, v26\n"
    "v_add_f32 v67, v67, v27\n"
    "s_mov_b64 exec, 1\n"
    "ds_add_u32 v87, v68 offset:4\n"
    "s_mov_b64 exec, -1\n"
    "v_mul_f32 v60, v60, v76\n"
    "v_mul_f32 v61, v61, v77\n"
    "v_mul_f32 v62, v62, v78\n"
    "v_mul_f32 v63, v63, v79\n"
    "v_mul_f32 v64, v64, v76\n"
    "v_mul_f32 v65, v65, v77\n"
    "v_mul_f32 v66, v66, v78\n"
    "v_mul_f32 v67, v67, v79\n"
    "v_cvt_pk_bf16_f32 v60, v60, v61\n"
    "v_cvt_pk_bf16_f32 v61, v62, v63\n"
    "v_cvt_pk_bf16_f32 v64, v64, v65\n"
    "v_cvt_pk_bf16_f32 v65, v66, v67\n"
    "global_store_dwordx2 v98, v[60:61], %[kt] sc1\n"
    "v_cndmask_b32 v64, v85, v64, %[mval]\n"
    "v_cndmask_b32 v65, v85, v65, %[mval]\n"
    "s_mov_b64 exec, %[mst]\n"
    "global_store_dwordx2 v99, v[64:65], %[kt] sc1\n"
    "s_mov_b64 exec, 0xff\n"
    "s_waitcnt vmcnt(0)\n"
    "global_store_dwordx2 v86, v[70:71], %[kf] sc1\n"
    "s_mov_b64 exec, -1\n"
    "KVE%=:\n"
    "s_setprio 0\n"
    "s_branch LKE%=\n"
    "LKA%=:\n"
    "s_setprio 2\n"
    "LKE%=:\n"
    "s_waitcnt lgkmcnt(0)\n"
      : "+{s[66:67]}"(epoch), "+{s[68:69]}"(sj), "+{v54}"(junk0), "+{v[0:31]}"(xr0), "+{v[32:39]}"(xr1), "+{v[84:87]}"(p84), "+{v[92:99]}"(p92), "+{v[110:113]}"(p110),
        "+{v[114:121]}"(p114), "+{v[122:123]}"(p122)
      : "{v[100:103]}"(p100), "{v[104:107]}"(p104), [wave] "s"(wave), [srd] "s"(srdq), [soff] "s"(qsrc & ~15u), [m0b] "s"(L0 + L_AR + 6144u * q), [wqp] "s"(wq),
        [srdk] "s"(srdk), [sofk] "s"(6400u * q), [m0k] "s"(L0 + q * PS + 20480u + 6144u), [m0c] "s"(L0 + q * PS + 20480u),
        [ty] "s"(ty), [q] "s"(q), [kt] "s"(kt), [kf] "s"(kf), [mval] "s"(mval), [mst] "s"(mst)
      : "v40", "v41", "v42", "v43", "v44", "v45", "v46", "v47", "v48", "v49", "v50", "v51", "v52", "v53", "v55", "v56", "v57", "v58", "v59", "v60", "v61", "v62", "v63", "v64", "v65", "v66", "v67", "v68", "v69", "v70", "v71", "v72", "v73", "v74", "v75", "v76", "v77", "v78", "v79", "a0", "a1", "a2", "a3", "a4", "a5", "a6", "a7", "a8", "a9", "a10", "a11", "a12", "a13", "a14", "a15", "a16", "a17", "a18", "a19", "a20", "a21", "a22", "a23", "a24", "a25", "a26", "a27", "a28", "a29", "a30", "a31", "a32", "a33", "a34", "a35", "a36", "a37", "a38", "a39", "a40", "a41", "a42", "a43", "a44", "a45", "a46", "a47", "a48", "a49", "a50", "a51", "a52", "a53", "a54", "a55", "a56", "a57", "a58", "a59", "a60", "a61", "a62", "a63", "a64", "a65", "a66", "a67", "a68", "a69", "a70", "a71", "a72", "a73", "a74", "a75", "a76", "a77", "a78", "a79", "a80", "a81", "a82", "a83", "a84", "a85", "a86", "a87", "a88", "a89", "a90", "a91", "a92", "a93", "a94", "a95", "a96", "a97", "a98", "a99", "a100", "a101", "a102", "a103", "a104", "a105", "a106", "a107", "a108", "a109", "a110", "a111", "a112", "a113", "a114", "a115", "a116", "a117", "a118", "a119", "s60", "s61", "s62", "s63", "s64", "s65", "vcc", "scc", "memory");
  }
  asm volatile("" ::: "memory");
  if (wave >= 4) {
    u64 epoch; asm volatile("" : "={s[66:67]}"(epoch));
    LANE_VARS
    XB_ADDR
    const int nun = (c < 6 && !(c & 1)) ? 4 : 3, un = (c < 6 ? (7 * c + 1) / 2 : 3 * c + 3) + q;
    const bool hasf = q < nun;
    const int q4 = (lane >> 2) & 3, p4 = lane & 3;
    const u32x4 srd1 = mksrd(w1, 2211840);
    const u32 m0b = rfl(L0 + q * PS);
    u32x8 g112; u32x4 g120;
#pragma unroll
    for (int i = 0; i < 3; ++i) g112[i] = lane * 16 + (5 * pp + 2 * ty + (i < 2 ? ty + i : (ty ? 0 : 2))) * 1024;
    g112[3] = lane * 16 + ty * 1024; g112[4] = lane * 16 + (1 - ty) * 1024;
#pragma unroll
    for (int gI = 0; gI < 4; ++gI) { const u32 a = m0b + gI * 1024 + ((hi * 32 + (r ^ ((2 * gI + hi) << 1))) << 4); if (gI < 3) g112[5 + gI] = a; else g120[0] = a; }
    g120[1] = L0 + L_FL + 8; g120[2] = (u32)(8 * q + (pp == q ? (1 - ty) * 4 + (lane & 3) : (lane & 7))) * 8u; g120[3] = (u32)(lane < 27 ? lane : 0) * 8u;
    u32x4 f108 = {m0b + 20480u + hi * 1024 + trl, (u32)(4 * hi + q4) * 3456u + (u32)(g4 & 1) * 64u + p4 * 16u, (u32)lane * 16u, (u32)(jj * 22 + (lane < 22 ? lane : 0)) * 128u};
    asm volatile("" : "+{v[112:119]}"(g112), "+{v[120:123]}"(g120), "+{v[100:107]}"(xbv), "+{v[108:111]}"(f108));
    u32 keep, tmp;
    if (q < 2) {
      lds_wait<1>(lds + L_FL + 60, 4u);
      poll2(ws + W_KVF + (b & 7) * 2048, q * 1024 + lane * 16, epoch);
      if (lane == 0) *(volatile LAS u32*)(lds + L_FL + 48 + 4 * q) = 1u;
    }
    lds_wait<1>(lds + L_FL + 12, 4u);
    lds_wait<1>(lds + L_FL + 4, 5u);
    __builtin_amdgcn_s_setprio(3);
    if (hasf) {
      u32 soff = 128u * un;
      asm volatile(
    "s_setprio 0\n"
    "s_cmp_gt_u32 %[q], 1\n"
    "s_cbranch_scc1 FT%=\n"
    "global_load_dword v64, v111, %[kvh] sc1\n"
    "FT%=:\n"
    "s_mov_b32 %[keep], m0\n"
    "s_add_u32 m0, %[m0b], 0\n"
    "s_nop 0\n"
    "buffer_load_dwordx4 v109, %[srd], %[soff] offen lds\n"
    "s_add_u32 m0, %[m0b], 1024\n"
    "s_add_u32 %[tmp], %[soff], 27648\n"
    "buffer_load_dwordx4 v109, %[srd], %[tmp] offen lds\n"
    "s_add_u32 %[soff], %[soff], 55296\n"
    "s_add_u32 m0, %[m0b], 2048\n"
    "s_nop 0\n"
    "buffer_load_dwordx4 v109, %[srd], %[soff] offen lds\n"
    "s_add_u32 m0, %[m0b], 3072\n"
    "s_add_u32 %[tmp], %[soff], 27648\n"
    "buffer_load_dwordx4 v109, %[srd], %[tmp] offen lds\n"
    "s_add_u32 %[soff], %[soff], 55296\n"
    "s_add_u32 m0, %[m0b], 4096\n"
    "s_nop 0\n"
    "buffer_load_dwordx4 v109, %[srd], %[soff] offen lds\n"
    "s_add_u32 m0, %[m0b], 5120\n"
    "s_add_u32 %[tmp], %[soff], 27648\n"
    "buffer_load_dwordx4 v109, %[srd], %[tmp] offen lds\n"
    "s_add_u32 %[soff], %[soff], 55296\n"
    "s_add_u32 m0, %[m0b], 6144\n"
    "s_nop 0\n"
    "buffer_load_dwordx4 v109, %[srd], %[soff] offen lds\n"
    "s_add_u32 m0, %[m0b], 7168\n"
    "s_add_u32 %[tmp], %[soff], 27648\n"
    "buffer_load_dwordx4 v109, %[srd], %[tmp] offen lds\n"
    "s_add_u32 %[soff], %[soff], 55296\n"
    "s_add_u32 m0, %[m0b], 8192\n"
    "s_nop 0\n"
    "buffer_load_dwordx4 v109, %[srd], %[soff] offen lds\n"
    "s_add_u32 m0, %[m0b], 9216\n"
    "s_add_u32 %[tmp], %[soff], 27648\n"
    "buffer_load_dwordx4 v109, %[srd], %[tmp] offen lds\n"
    "s_add_u32 %[soff], %[soff], 55296\n"
    "s_add_u32 m0, %[m0b], 10240\n"
    "s_nop 0\n"
    "buffer_load_dwordx4 v109, %[srd], %[soff] offen lds\n"
    "s_add_u32 m0, %[m0b], 11264\n"
    "s_add_u32 %[tmp], %[soff], 27648\n"
    "buffer_load_dwordx4 v109, %[srd], %[tmp] offen lds\n"
    "s_add_u32 %[soff], %[soff], 55296\n"
    "s_waitcnt vmcnt(10)\n"
    "ds_read_b64_tr_b16 a[0:1], v108 offset:0\n"
    "ds_read_b64_tr_b16 a[2:3], v108 offset:512\n"
    "ds_read_b64_tr_b16 a[4:5], v108 offset:256\n"
    "ds_read_b64_tr_b16 a[6:7], v108 offset:768\n"
    "ds_read_b128 a[16:19], v100 offset:0\n"
    "ds_read_b128 a[20:23], v104 offset:0\n"
    "s_waitcnt lgkmcnt(0)\n"
    "v_mfma_f32_32x32x16_bf16 v[0:15], a[0:3], a[16:19], 0\n"
    "s_waitcnt vmcnt(8)\n"
    "ds_read_b64_tr_b16 a[8:9], v108 offset:2048\n"
    "ds_read_b64_tr_b16 a[10:11], v108 offset:2560\n"
    "ds_read_b64_tr_b16 a[12:13], v108 offset:2304\n"
    "v_mfma_f32_32x32x16_bf16 v[16:31], a[0:3], a[20:23], 0\n"
    "ds_read_b64_tr_b16 a[14:15], v108 offset:2816\n"
    "ds_read_b128 a[24:27], v101 offset:1024\n"
    "ds_read_b128 a[28:31], v105 offset:1024\n"
    "s_add_u32 m0, %[m0b], 0\n"
    "s_nop 0\n"
    "buffer_load_dwordx4 v109, %[srd], %[soff] offen lds\n"
    "v_mfma_f32_32x32x16_bf16 v[32:47], a[4:7], a[16:19], 0\n"
    "s_add_u32 m0, %[m0b], 1024\n"
    "s_add_u32 %[tmp], %[soff], 27648\n"
    "buffer_load_dwordx4 v109, %[srd], %[tmp] offen lds\n"
    "s_add_u32 %[soff], %[soff], 55296\n"
    "v_mfma_f32_32x32x16_bf16 v[48:63], a[4:7], a[20:23], 0\n"
    "s_waitcnt lgkmcnt(0)\n"
    "v_mfma_f32_32x32x16_bf16 v[0:15], a[8:11], a[24:27], v[0:15]\n"
    "s_waitcnt vmcnt(8)\n"
    "ds_read_b64_tr_b16 a[0:1], v108 offset:4096\n"
    "ds_read_b64_tr_b16 a[2:3], v108 offset:4608\n"
    "ds_read_b64_tr_b16 a[4:5], v108 offset:4352\n"
    "v_mfma_f32_32x32x16_bf16 v[16:31], a[8:11], a[28:31], v[16:31]\n"
    "ds_read_b64_tr_b16 a[6:7], v108 offset:4864\n"
    "ds_read_b128 a[16:19], v102 offset:2048\n"
    "ds_read_b128 a[20:23], v106 offset:2048\n"
    "s_add_u32 m0, %[m0b], 2048\n"
    "s_nop 0\n"
    "buffer_load_dwordx4 v109, %[srd], %[soff] offen lds\n"
    "v_mfma_f32_32x32x16_bf16 v[32:47], a[12:15], a[24:27], v[32:47]\n"
    "s_add_u32 m0, %[m0b], 3072\n"
    "s_add_u32 %[tmp], %[soff], 27648\n"
    "buffer_load_dwordx4 v109, %[srd], %[tmp] offen lds\n"
    "s_add_u32 %[soff], %[soff], 55296\n"
    "v_mfma_f32_32x32x16_bf16 v[48:63], a[12:15], a[28:31], v[48:63]\n"
    "s_waitcnt lgkmcnt(0)\n"
    "v_mfma_f32_32x32x16_bf16 v[0:15], a[0:3], a[16:19], v[0:15]\n"
    "s_waitcnt vmcnt(8)\n"
    "ds_read_b64_tr_b16 a[8:9], v108 offset:6144\n"
    "ds_read_b64_tr_b16 a[10:11], v108 offset:6656\n"
    "ds_read_b64_tr_b16 a[12:13], v108 offset:6400\n"
    "v_mfma_f32_32x32x16_bf16 v[16:31], a[0:3], a[20:23], v[16:31]\n"
    "ds_read_b64_tr_b16 a[14:15], v108 offset:6912\n"
    "ds_read_b128 a[24:27], v103 offset:3072\n"
    "ds_read_b128 a[28:31], v107 offset:3072\n"
    "s_add_u32 m0, %[m0b], 4096\n"
    "s_nop 0\n"
    "buffer_load_dwordx4 v109, %[srd], %[soff] offen lds\n"
    "v_mfma_f32_32x32x16_bf16 v[32:47], a[4:7], a[16:19], v[32:47]\n"
    "s_add_u32 m0, %[m0b], 5120\n"
    "s_add_u32 %[tmp], %[soff], 27648\n"
    "buffer_load_dwordx4 v109, %[srd], %[tmp] offen lds\n"
    "s_add_u32 %[soff], %[soff], 55296\n"
    "v_mfma_f32_32x32x16_bf16 v[48:63], a[4:7], a[20:23], v[48:63]\n"
    "s_waitcnt lgkmcnt(0)\n"
    "v_mfma_f32_32x32x16_bf16 v[0:15], a[8:11], a[24:27], v[0:15]\n"
    "s_waitcnt vmcnt(8)\n"
    "ds_read_b64_tr_b16 a[0:1], v108 offset:8192\n"
    "ds_read_b64_tr_b16 a[2:3], v108 offset:8704\n"
    "ds_read_b64_tr_b16 a[4:5], v108 offset:8448\n"
    "v_mfma_f32_32x32x16_bf16 v[16:31], a[8:11], a[28:31], v[16:31]\n"
    "ds_read_b64_tr_b16 a[6:7], v108 offset:8960\n"
    "ds_read_b128 a[16:19], v100 offset:4096\n"
    "ds_read_b128 a[20:23], v104 offset:4096\n"
    "s_add_u32 m0, %[m0b], 6144\n"
    "s_nop 0\n"
    "buffer_load_dwordx4 v109, %[srd], %[soff] offen lds\n"
    "v_mfma_f32_32x32x16_bf16 v[32:47], a[12:15], a[24:27], v[32:47]\n"
    "s_add_u32 m0, %[m0b], 7168\n"
    "s_add_u32 %[tmp], %[soff], 27648\n"
    "buffer_load_dwordx4 v109, %[srd], %[tmp] offen lds\n"
    "s_add_u32 %[soff], %[soff], 55296\n"
    "v_mfma_f32_32x32x16_bf16 v[48:63], a[12:15], a[28:31], v[48:63]\n"
    "s_waitcnt lgkmcnt(0)\n"
    "v_mfma_f32_32x32x16_bf16 v[0:15], a[0:3], a[16:19], v[0:15]\n"
    "s_waitcnt vmcnt(8)\n"
    "ds_read_b64_tr_b16 a[8:9], v108 offset:10240\n"
    "ds_read_b64_tr_b16 a[10:11], v108 offset:10752\n"
    "ds_read_b64_tr_b16 a[12:13], v108 offset:10496\n"
    "v_mfma_f32_32x32x16_bf16 v[16:31], a[0:3], a[20:23], v[16:31]\n"
    "ds_read_b64_tr_b16 a[14:15], v108 offset:11008\n"
    "ds_read_b128 a[24:27], v101 offset:5120\n"
    "ds_read_b128 a[28:31], v105 offset:5120\n"
    "s_add_u32 m0, %[m0b], 8192\n"
    "s_nop 0\n"
    "buffer_load_dwordx4 v109, %[srd], %[soff] offen lds\n"
    "v_mfma_f32_32x32x16_bf16 v[32:47], a[4:7], a[16:19], v[32:47]\n"
    "s_add_u32 m0, %[m0b], 9216\n"
    "s_add_u32 %[tmp], %[soff], 27648\n"
    "buffer_load_dwordx4 v109, %[srd], %[tmp] offen lds\n"
    "s_add_u32 %[soff], %[soff], 55296\n"
    "v_mfma_f32_32x32x16_bf16 v[48:63], a[4:7], a[20:23], v[48:63]\n"
    "s_waitcnt lgkmcnt(0)\n"
    "v_mfma_f32_32x32x16_bf16 v[0:15], a[8:11], a[24:27], v[0:15]\n"
    "s_waitcnt vmcnt(8)\n"
    "ds_read_b64_tr_b16 a[0:1], v108 offset:0\n"
    "ds_read_b64_tr_b16 a[2:3], v108 offset:512\n"
    "ds_read_b64_tr_b16 a[4:5], v108 offset:256\n"
    "v_mfma_f32_32x32x16_bf16 v[16:31], a[8:11], a[28:31], v[16:31]\n"
    "ds_read_b64_tr_b16 a[6:7], v108 offset:768\n"
    "ds_read_b128 a[16:19], v102 offset:6144\n"
    "ds_read_b128 a[20:23], v106 offset:6144\n"
    "s_add_u32 m0, %[m0b], 10240\n"
    "s_nop 0\n"
    "buffer_load_dwordx4 v109, %[srd], %[soff] offen lds\n"
    "v_mfma_f32_32x32x16_bf16 v[32:47], a[12:15], a[24:27], v[32:47]\n"
    "s_add_u32 m0, %[m0b], 11264\n"
    "s_add_u32 %[tmp], %[soff], 27648\n"
    "buffer_load_dwordx4 v109, %[srd], %[tmp] offen lds\n"
    "s_add_u32 %[soff], %[soff], 55296\n"
    "v_mfma_f32_32x32x16_bf16 v[48:63], a[12:15], a[28:31], v[48:63]\n"
    "s_waitcnt lgkmcnt(0)\n"
    "v_mfma_f32_32x32x16_bf16 v[0:15], a[0:3], a[16:19], v[0:15]\n"
    "s_waitcnt vmcnt(8)\n"
    "ds_read_b64_tr_b16 a[8:9], v108 offset:2048\n"
    "ds_read_b64_tr_b16 a[10:11], v108 offset:2560\n"
    "ds_read_b64_tr_b16 a[12:13], v108 offset:2304\n"
    "v_mfma_f32_32x32x16_bf16 v[16:31], a[0:3], a[20:23], v[16:31]\n"
    "ds_read_b64_tr_b16 a[14:15], v108 offset:2816\n"
    "ds_read_b128 a[24:27], v103 offset:7168\n"
    "ds_read_b128 a[28:31], v107 offset:7168\n"
    "s_add_u32 m0, %[m0b], 0\n"
    "s_nop 0\n"
    "buffer_load_dwordx4 v109, %[srd], %[soff] offen lds\n"
    "v_mfma_f32_32x32x16_bf16 v[32:47], a[4:7], a[16:19], v[32:47]\n"
    "s_add_u32 m0, %[m0b], 1024\n"
    "s_add_u32 %[tmp], %[soff], 27648\n"
    "buffer_load_dwordx4 v109, %[srd], %[tmp] offen lds\n"
    "s_add_u32 %[soff], %[soff], 55296\n"
    "v_mfma_f32_32x32x16_bf16 v[48:63], a[4:7], a[20:23], v[48:63]\n"
    "s_waitcnt lgkmcnt(0)\n"
    "v_mfma_f32_32x32x16_bf16 v[0:15], a[8:11], a[24:27], v[0:15]\n"
    "s_waitcnt vmcnt(8)\n"
    "ds_read_b64_tr_b16 a[0:1], v108 offset:4096\n"
    "ds_read_b64_tr_b16 a[2:3], v108 offset:4608\n"
    "ds_read_b64_tr_b16 a[4:5], v108 offset:4352\n"
    "v_mfma_f32_32x32x16_bf16 v[16:31], a[8:11], a[28:31], v[16:31]\n"
    "ds_read_b64_tr_b16 a[6:7], v108 offset:4864\n"
    "ds_read_b128 a[16:19], v100 offset:8192\n"
    "ds_read_b128 a[20:23], v104 offset:8192\n"
    "s_add_u32 m0, %[m0b], 2048\n"
    "s_nop 0\n"
    "buffer_load_dwordx4 v109, %[srd], %[soff] offen lds\n"
    "v_mfma_f32_32x32x16_bf16 v[32:47], a[12:15], a[24:27], v[32:47]\n"
    "s_add_u32 m0, %[m0b], 3072\n"
    "s_add_u32 %[tmp], %[soff], 27648\n"
    "buffer_load_dwordx4 v109, %[srd], %[tmp] offen lds\n"
    "s_add_u32 %[soff], %[soff], 55296\n"
    "v_mfma_f32_32x32x16_bf16 v[48:63], a[12:15], a[28:31], v[48:63]\n"
    "s_waitcnt lgkmcnt(0)\n"
    "v_mfma_f32_32x32x16_bf16 v[0:15], a[0:3], a[16:19], v[0:15]\n"
    "s_waitcnt vmcnt(8)\n"
    "ds_read_b64_tr_b16 a[8:9], v108 offset:6144\n"
    "ds_read_b64_tr_b16 a[10:11], v108 offset:6656\n"
    "ds_read_b64_tr_b16 a[12:13], v108 offset:6400\n"
    "v_mfma_f32_32x32x16_bf16 v[16:31], a[0:3], a[20:23], v[16:31]\n"
    "ds_read_b64_tr_b16 a[14:15], v108 offset:6912\n"
    "ds_read_b128 a[24:27], v101 offset:9216\n"
    "ds_read_b128 a[28:31], v105 offset:9216\n"
    "s_add_u32 m0, %[m0b], 4096\n"
    "s_nop 0\n"
    "buffer_load_dwordx4 v109, %[srd], %[soff] offen lds\n"
    "v_mfma_f32_32x32x16_bf16 v[32:47], a[4:7], a[16:19], v[32:47]\n"
    "s_add_u32 m0, %[m0b], 5120\n"
    "s_add_u32 %[tmp], %[soff], 27648\n"
    "buffer_load_dwordx4 v109, %[srd], %[tmp] offen lds\n"
    "s_add_u32 %[soff], %[soff], 55296\n"
    "v_mfma_f32_32x32x16_bf16 v[48:63], a[4:7], a[20:23], v[48:63]\n"
    "s_waitcnt lgkmcnt(0)\n"
    "v_mfma_f32_32x32x16_bf16 v[0:15], a[8:11], a[24:27], v[0:15]\n"
    "s_waitcnt vmcnt(8)\n"
    "ds_read_b64_tr_b16 a[0:1], v108 offset:8192\n"
    "ds_read_b64_tr_b16 a[2:3], v108 offset:8704\n"
    "ds_read_b64_tr_b16 a[4:5], v108 offset:8448\n"
    "v_mfma_f32_32x32x16_bf16 v[16:31], a[8:11], a[28:31], v[16:31]\n"
    "ds_read_b64_tr_b16 a[6:7], v108 offset:8960\n"
    "ds_read_b128 a[16:19], v102 offset:10240\n"
    "ds_read_b128 a[20:23], v106 offset:10240\n"
    "s_add_u32 m0, %[m0b], 6144\n"
    "s_nop 0\n"
    "buffer_load_dwordx4 v109, %[srd], %[soff] offen lds\n"
    "v_mfma_f32_32x32x16_bf16 v[32:47], a[12:15], a[24:27], v[32:47]\n"
    "s_add_u32 m0, %[m0b], 7168\n"
    "s_add_u32 %[tmp], %[soff], 27648\n"
    "buffer_load_dwordx4 v109, %[srd], %[tmp] offen lds\n"
    "s_add_u32 %[soff], %[soff], 55296\n"
    "v_mfma_f32_32x32x16_bf16 v[48:63], a[12:15], a[28:31], v[48:63]\n"
    "s_waitcnt lgkmcnt(0)\n"
    "v_mfma_f32_32x32x16_bf16 v[0:15], a[0:3], a[16:19], v[0:15]\n"
    "s_waitcnt vmcnt(8)\n"
    "ds_read_b64_tr_b16 a[8:9], v108 offset:10240\n"
    "ds_read_b64_tr_b16 a[10:11], v108 offset:10752\n"
    "ds_read_b64_tr_b16 a[12:13], v108 offset:10496\n"
    "v_mfma_f32_32x32x16_bf16 v[16:31], a[0:3], a[20:23], v[16:31]\n"
    "ds_read_b64_tr_b16 a[14:15], v108 offset:11008\n"
    "ds_read_b128 a[24:27], v103 offset:11264\n"
    "ds_read_b128 a[28:31], v107 offset:11264\n"
    "s_add_u32 m0, %[m0b], 8192\n"
    "s_nop 0\n"
    "buffer_load_dwordx4 v109, %[srd], %[soff] offen lds\n"
    "v_mfma_f32_32x32x16_bf16 v[32:47], a[4:7], a[16:19], v[32:47]\n"
    "s_add_u32 m0, %[m0b], 9216\n"
    "s_add_u32 %[tmp], %[soff], 27648\n"
    "buffer_load_dwordx4 v109, %[srd], %[tmp] offen lds\n"
    "s_add_u32 %[soff], %[soff], 55296\n"
    "v_mfma_f32_32x32x16_bf16 v[48:63], a[4:7], a[20:23], v[48:63]\n"
    "s_waitcnt lgkmcnt(0)\n"
    "v_mfma_f32_32x32x16_bf16 v[0:15], a[8:11], a[24:27], v[0:15]\n"
    "s_waitcnt vmcnt(8)\n"
    "ds_read_b64_tr_b16 a[0:1], v108 offset:0\n"
    "ds_read_b64_tr_b16 a[2:3], v108 offset:512\n"
    "ds_read_b64_tr_b16 a[4:5], v108 offset:256\n"
    "v_mfma_f32_32x32x16_bf16 v[16:31], a[8:11], a[28:31], v[16:31]\n"
    "ds_read_b64_tr_b16 a[6:7], v108 offset:768\n"
    "ds_read_b128 a[16:19], v100 offset:12288\n"
    "ds_read_b128 a[20:23], v104 offset:12288\n"
    "s_add_u32 m0, %[m0b], 10240\n"
    "s_nop 0\n"
    "buffer_load_dwordx4 v109, %[srd], %[soff] offen lds\n"
    "v_mfma_f32_32x32x16_bf16 v[32:47], a[12:15], a[24:27], v[32:47]\n"
    "s_add_u32 m0, %[m0b], 11264\n"
    "s_add_u32 %[tmp], %[soff], 27648\n"
    "buffer_load_dwordx4 v109, %[srd], %[tmp] offen lds\n"
    "s_add_u32 %[soff], %[soff], 55296\n"
    "v_mfma_f32_32x32x16_bf16 v[48:63], a[12:15], a[28:31], v[48:63]\n"
    "s_waitcnt lgkmcnt(0)\n"
    "v_mfma_f32_32x32x16_bf16 v[0:15], a[0:3], a[16:19], v[0:15]\n"
    "s_waitcnt vmcnt(8)\n"
    "ds_read_b64_tr_b16 a[8:9], v108 offset:2048\n"
    "ds_read_b64_tr_b16 a[10:11], v108 offset:2560\n"
    "ds_read_b64_tr_b16 a[12:13], v108 offset:2304\n"
    "v_mfma_f32_32x32x16_bf16 v[16:31], a[0:3], a[20:23], v[16:31]\n"
    "ds_read_b64_tr_b16 a[14:15], v108 offset:2816\n"
    "ds_read_b128 a[24:27], v101 offset:13312\n"
    "ds_read_b128 a[28:31], v105 offset:13312\n"
    "s_add_u32 m0, %[m0b], 0\n"
    "s_nop 0\n"
    "buffer_load_dwordx4 v109, %[srd], %[soff] offen lds\n"
    "v_mfma_f32_32x32x16_bf16 v[32:47], a[4:7], a[16:19], v[32:47]\n"
    "s_add_u32 m0, %[m0b], 1024\n"
    "s_add_u32 %[tmp], %[soff], 27648\n"
    "buffer_load_dwordx4 v109, %[srd], %[tmp] offen lds\n"
    "s_add_u32 %[soff], %[soff], 55296\n"
    "v_mfma_f32_32x32x16_bf16 v[48:63], a[4:7], a[20:23], v[48:63]\n"
    "s_waitcnt lgkmcnt(0)\n"
    "v_mfma_f32_32x32x16_bf16 v[0:15], a[8:11], a[24:27], v[0:15]\n"
    "s_waitcnt vmcnt(8)\n"
    "ds_read_b64_tr_b16 a[0:1], v108 offset:4096\n"
    "ds_read_b64_tr_b16 a[2:3], v108 offset:4608\n"
    "ds_read_b64_tr_b16 a[4:5], v108 offset:4352\n"
    "v_mfma_f32_32x32x16_bf16 v[16:31], a[8:11], a[28:31], v[16:31]\n"
    "ds_read_b64_tr_b16 a[6:7], v108 offset:4864\n"
    "ds_read_b128 a[16:19], v102 offset:14336\n"
    "ds_read_b128 a[20:23], v106 offset:14336\n"
    "s_add_u32 m0, %[m0b], 2048\n"
    "s_nop 0\n"
    "buffer_load_dwordx4 v109, %[srd], %[soff] offen lds\n"
    "v_mfma_f32_32x32x16_bf16 v[32:47], a[12:15], a[24:27], v[32:47]\n"
    "s_add_u32 m0, %[m0b], 3072\n"
    "s_add_u32 %[tmp], %[soff], 27648\n"
    "buffer_load_dwordx4 v109, %[srd], %[tmp] offen lds\n"
    "s_add_u32 %[soff], %[soff], 55296\n"
    "v_mfma_f32_32x32x16_bf16 v[48:63], a[12:15], a[28:31], v[48:63]\n"
    "s_waitcnt lgkmcnt(0)\n"
    "v_mfma_f32_32x32x16_bf16 v[0:15], a[0:3], a[16:19], v[0:15]\n"
    "s_waitcnt vmcnt(8)\n"
    "ds_read_b64_tr_b16 a[8:9], v108 offset:6144\n"
    "ds_read_b64_tr_b16 a[10:11], v108 offset:6656\n"
    "ds_read_b64_tr_b16 a[12:13], v108 offset:6400\n"
    "v_mfma_f32_32x32x16_bf16 v[16:31], a[0:3], a[20:23], v[16:31]\n"
    "ds_read_b64_tr_b16 a[14:15], v108 offset:6912\n"
    "ds_read_b128 a[24:27], v103 offset:15360\n"
    "ds_read_b128 a[28:31], v107 offset:15360\n"
    "s_add_u32 m0, %[m0b], 4096\n"
    "s_nop 0\n"
    "buffer_load_dwordx4 v109, %[srd], %[soff] offen lds\n"
    "v_mfma_f32_32x32x16_bf16 v[32:47], a[4:7], a[16:19], v[32:47]\n"
    "s_add_u32 m0, %[m0b], 5120\n"
    "s_add_u32 %[tmp], %[soff], 27648\n"
    "buffer_load_dwordx4 v109, %[srd], %[tmp] offen lds\n"
    "s_add_u32 %[soff], %[soff], 55296\n"
    "v_mfma_f32_32x32x16_bf16 v[48:63], a[4:7], a[20:23], v[48:63]\n"
    "s_waitcnt lgkmcnt(0)\n"
    "v_mfma_f32_32x32x16_bf16 v[0:15], a[8:11], a[24:27], v[0:15]\n"
    "s_waitcnt vmcnt(8)\n"
    "ds_read_b64_tr_b16 a[0:1], v108 offset:8192\n"
    "ds_read_b64_tr_b16 a[2:3], v108 offset:8704\n"
    "ds_read_b64_tr_b16 a[4:5], v108 offset:8448\n"
    "v_mfma_f32_32x32x16_bf16 v[16:31], a[8:11], a[28:31], v[16:31]\n"
    "ds_read_b64_tr_b16 a[6:7], v108 offset:8960\n"
    "ds_read_b128 a[16:19], v100 offset:16384\n"
    "ds_read_b128 a[20:23], v104 offset:16384\n"
    "s_add_u32 m0, %[m0b], 6144\n"
    "s_nop 0\n"
    "buffer_load_dwordx4 v109, %[srd], %[soff] offen lds\n"
    "v_mfma_f32_32x32x16_bf16 v[32:47], a[12:15], a[24:27], v[32:47]\n"
    "s_add_u32 m0, %[m0b], 7168\n"
    "s_add_u32 %[tmp], %[soff], 27648\n"
    "buffer_load_dwordx4 v109, %[srd], %[tmp] offen lds\n"
    "s_add_u32 %[soff], %[soff], 55296\n"
    "v_mfma_f32_32x32x16_bf16 v[48:63], a[12:15], a[28:31], v[48:63]\n"
    "s_waitcnt lgkmcnt(0)\n"
    "v_mfma_f32_32x32x16_bf16 v[0:15], a[0:3], a[16:19], v[0:15]\n"
    "s_waitcnt vmcnt(8)\n"
    "ds_read_b64_tr_b16 a[8:9], v108 offset:10240\n"
    "ds_read_b64_tr_b16 a[10:11], v108 offset:10752\n"
    "ds_read_b64_tr_b16 a[12:13], v108 offset:10496\n"
    "v_mfma_f32_32x32x16_bf16 v[16:31], a[0:3], a[20:23], v[16:31]\n"
    "ds_read_b64_tr_b16 a[14:15], v108 offset:11008\n"
    "ds_read_b128 a[24:27], v101 offset:17408\n"
    "ds_read_b128 a[28:31], v105 offset:17408\n"
    "s_add_u32 m0, %[m0b], 8192\n"
    "s_nop 0\n"
    "buffer_load_dwordx4 v109, %[srd], %[soff] offen lds\n"
    "v_mfma_f32_32x32x16_bf16 v[32:47], a[4:7], a[16:19], v[32:47]\n"
    "s_add_u32 m0, %[m0b], 9216\n"
    "s_add_u32 %[tmp], %[soff], 27648\n"
    "buffer_load_dwordx4 v109, %[srd], %[tmp] offen lds\n"
    "s_add_u32 %[soff], %[soff], 55296\n"
    "v_mfma_f32_32x32x16_bf16 v[48:63], a[4:7], a[20:23], v[48:63]\n"
    "s_waitcnt lgkmcnt(0)\n"
    "v_mfma_f32_32x32x16_bf16 v[0:15], a[8:11], a[24:27], v[0:15]\n"
    "s_waitcnt vmcnt(8)\n"
    "ds_read_b64_tr_b16 a[0:1], v108 offset:0\n"
    "ds_read_b64_tr_b16 a[2:3], v108 offset:512\n"
    "ds_read_b64_tr_b16 a[4:5], v108 offset:256\n"
    "v_mfma_f32_32x32x16_bf16 v[16:31], a[8:11], a[28:31], v[16:31]\n"
    "ds_read_b64_tr_b16 a[6:7], v108 offset:768\n"
    "ds_read_b128 a[16:19], v102 offset:18432\n"
    "ds_read_b128 a[20:23], v106 offset:18432\n"
    "s_add_u32 m0, %[m0b], 10240\n"
    "s_nop 0\n"
    "buffer_load_dwordx4 v109, %[srd], %[soff] offen lds\n"
    "v_mfma_f32_32x32x16_bf16 v[32:47], a[12:15], a[24:27], v[32:47]\n"
    "s_add_u32 m0, %[m0b], 11264\n"
    "s_add_u32 %[tmp], %[soff], 27648\n"
    "buffer_load_dwordx4 v109, %[srd], %[tmp] offen lds\n"
    "s_add_u32 %[soff], %[soff], 55296\n"
    "v_mfma_f32_32x32x16_bf16 v[48:63], a[12:15], a[28:31], v[48:63]\n"
    "s_waitcnt lgkmcnt(0)\n"
    "v_mfma_f32_32x32x16_bf16 v[0:15], a[0:3], a[16:19], v[0:15]\n"
    "s_waitcnt vmcnt(8)\n"
    "ds_read_b64_tr_b16 a[8:9], v108 offset:2048\n"
    "ds_read_b64_tr_b16 a[10:11], v108 offset:2560\n"
    "ds_read_b64_tr_b16 a[12:13], v108 offset:2304\n"
    "v_mfma_f32_32x32x16_bf16 v[16:31], a[0:3], a[20:23], v[16:31]\n"
    "ds_read_b64_tr_b16 a[14:15], v108 offset:2816\n"
    "ds_read_b128 a[24:27], v103 offset:19456\n"
    "ds_read_b128 a[28:31], v107 offset:19456\n"
    "s_add_u32 m0, %[m0b], 0\n"
    "s_nop 0\n"
    "buffer_load_dwordx4 v109, %[srd], %[soff] offen lds\n"
    "v_mfma_f32_32x32x16_bf16 v[32:47], a[4:7], a[16:19], v[32:47]\n"
    "s_add_u32 m0, %[m0b], 1024\n"
    "s_add_u32 %[tmp], %[soff], 27648\n"
    "buffer_load_dwordx4 v109, %[srd], %[tmp] offen lds\n"
    "s_add_u32 %[soff], %[soff], 55296\n"
    "v_mfma_f32_32x32x16_bf16 v[48:63], a[4:7], a[20:23], v[48:63]\n"
    "s_waitcnt lgkmcnt(0)\n"
    "v_mfma_f32_32x32x16_bf16 v[0:15], a[8:11], a[24:27], v[0:15]\n"
    "s_waitcnt vmcnt(8)\n"
    "ds_read_b64_tr_b16 a[0:1], v108 offset:4096\n"
    "ds_read_b64_tr_b16 a[2:3], v108 offset:4608\n"
    "ds_read_b64_tr_b16 a[4:5], v108 offset:4352\n"
    "v_mfma_f32_32x32x16_bf16 v[16:31], a[8:11], a[28:31], v[16:31]\n"
    "ds_read_b64_tr_b16 a[6:7], v108 offset:4864\n"
    "ds_read_b128 a[16:19], v100 offset:34752\n"
    "ds_read_b128 a[20:23], v104 offset:34752\n"
    "s_add_u32 m0, %[m0b], 2048\n"
    "s_nop 0\n"
    "buffer_load_dwordx4 v109, %[srd], %[soff] offen lds\n"
    "v_mfma_f32_32x32x16_bf16 v[32:47], a[12:15], a[24:27], v[32:47]\n"
    "s_add_u32 m0, %[m0b], 3072\n"
    "s_add_u32 %[tmp], %[soff], 27648\n"
    "buffer_load_dwordx4 v109, %[srd], %[tmp] offen lds\n"
    "s_add_u32 %[soff], %[soff], 55296\n"
    "v_mfma_f32_32x32x16_bf16 v[48:63], a[12:15], a[28:31], v[48:63]\n"
    "s_waitcnt lgkmcnt(0)\n"
    "v_mfma_f32_32x32x16_bf16 v[0:15], a[0:3], a[16:19], v[0:15]\n"
    "s_waitcnt vmcnt(8)\n"
    "ds_read_b64_tr_b16 a[8:9], v108 offset:6144\n"
    "ds_read_b64_tr_b16 a[10:11], v108 offset:6656\n"
    "ds_read_b64_tr_b16 a[12:13], v108 offset:6400\n"
    "v_mfma_f32_32x32x16_bf16 v[16:31], a[0:3], a[20:23], v[16:31]\n"
    "ds_read_b64_tr_b16 a[14:15], v108 offset:6912\n"
    "ds_read_b128 a[24:27], v101 offset:35776\n"
    "ds_read_b128 a[28:31], v105 offset:35776\n"
    "s_add_u32 m0, %[m0b], 4096\n"
    "s_nop 0\n"
    "buffer_load_dwordx4 v109, %[srd], %[soff] offen lds\n"
    "v_mfma_f32_32x32x16_bf16 v[32:47], a[4:7], a[16:19], v[32:47]\n"
    "s_add_u32 m0, %[m0b], 5120\n"
    "s_add_u32 %[tmp], %[soff], 27648\n"
    "buffer_load_dwordx4 v109, %[srd], %[tmp] offen lds\n"
    "s_add_u32 %[soff], %[soff], 55296\n"
    "v_mfma_f32_32x32x16_bf16 v[48:63], a[4:7], a[20:23], v[48:63]\n"
    "s_waitcnt lgkmcnt(0)\n"
    "v_mfma_f32_32x32x16_bf16 v[0:15], a[8:11], a[24:27], v[0:15]\n"
    "s_waitcnt vmcnt(8)\n"
    "ds_read_b64_tr_b16 a[0:1], v108 offset:8192\n"
    "ds_read_b64_tr_b16 a[2:3], v108 offset:8704\n"
    "ds_read_b64_tr_b16 a[4:5], v108 offset:8448\n"
    "v_mfma_f32_32x32x16_bf16 v[16:31], a[8:11], a[28:31], v[16:31]\n"
    "ds_read_b64_tr_b16 a[6:7], v108 offset:8960\n"
    "ds_read_b128 a[16:19], v102 offset:36800\n"
    "ds_read_b128 a[20:23], v106 offset:36800\n"
    "s_add_u32 m0, %[m0b], 6144\n"
    "s_nop 0\n"
    "buffer_load_dwordx4 v109, %[srd], %[soff] offen lds\n"
    "v_mfma_f32_32x32x16_bf16 v[32:47], a[12:15], a[24:27], v[32:47]\n"
    "s_add_u32 m0, %[m0b], 7168\n"
    "s_add_u32 %[tmp], %[soff], 27648\n"
    "buffer_load_dwordx4 v109, %[srd], %[tmp] offen lds\n"
    "s_add_u32 %[soff], %[soff], 55296\n"
    "v_mfma_f32_32x32x16_bf16 v[48:63], a[12:15], a[28:31], v[48:63]\n"
    "s_waitcnt lgkmcnt(0)\n"
    "v_mfma_f32_32x32x16_bf16 v[0:15], a[0:3], a[16:19], v[0:15]\n"
    "s_waitcnt vmcnt(8)\n"
    "ds_read_b64_tr_b16 a[8:9], v108 offset:10240\n"
    "ds_read_b64_tr_b16 a[10:11], v108 offset:10752\n"
    "ds_read_b64_tr_b16 a[12:13], v108 offset:10496\n"
    "v_mfma_f32_32x32x16_bf16 v[16:31], a[0:3], a[20:23], v[16:31]\n"
    "ds_read_b64_tr_b16 a[14:15], v108 offset:11008\n"
    "ds_read_b128 a[24:27], v103 offset:37824\n"
    "ds_read_b128 a[28:31], v107 offset:37824\n"
    "s_add_u32 m0, %[m0b], 8192\n"
    "s_nop 0\n"
    "buffer_load_dwordx4 v109, %[srd], %[soff] offen lds\n"
    "v_mfma_f32_32x32x16_bf16 v[32:47], a[4:7], a[16:19], v[32:47]\n"
    "s_add_u32 m0, %[m0b], 9216\n"
    "s_add_u32 %[tmp], %[soff], 27648\n"
    "buffer_load_dwordx4 v109, %[srd], %[tmp] offen lds\n"
    "s_add_u32 %[soff], %[soff], 55296\n"
    "v_mfma_f32_32x32x16_bf16 v[48:63], a[4:7], a[20:23], v[48:63]\n"
    "s_waitcnt lgkmcnt(0)\n"
    "v_mfma_f32_32x32x16_bf16 v[0:15], a[8:11], a[24:27], v[0:15]\n"
    "s_waitcnt vmcnt(8)\n"
    "ds_read_b64_tr_b16 a[0:1], v108 offset:0\n"
    "ds_read_b64_tr_b16 a[2:3], v108 offset:512\n"
    "ds_read_b64_tr_b16 a[4:5], v108 offset:256\n"
    "v_mfma_f32_32x32x16_bf16 v[16:31], a[8:11], a[28:31], v[16:31]\n"
    "ds_read_b64_tr_b16 a[6:7], v108 offset:768\n"
    "ds_read_b128 a[16:19], v100 offset:38848\n"
    "ds_read_b128 a[20:23], v104 offset:38848\n"
    "s_add_u32 m0, %[m0b], 10240\n"
    "s_nop 0\n"
    "buffer_load_dwordx4 v109, %[srd], %[soff] offen lds\n"
    "v_mfma_f32_32x32x16_bf16 v[32:47], a[12:15], a[24:27], v[32:47]\n"
    "s_add_u32 m0, %[m0b], 11264\n"
    "s_add_u32 %[tmp], %[soff], 27648\n"
    "buffer_load_dwordx4 v109, %[srd], %[tmp] offen lds\n"
    "s_add_u32 %[soff], %[soff], 55296\n"
    "v_mfma_f32_32x32x16_bf16 v[48:63], a[12:15], a[28:31], v[48:63]\n"
    "s_waitcnt lgkmcnt(0)\n"
    "v_mfma_f32_32x32x16_bf16 v[0:15], a[0:3], a[16:19], v[0:15]\n"
    "s_waitcnt vmcnt(8)\n"
    "ds_read_b64_tr_b16 a[8:9], v108 offset:2048\n"
    "ds_read_b64_tr_b16 a[10:11], v108 offset:2560\n"
    "ds_read_b64_tr_b16 a[12:13], v108 offset:2304\n"
    "v_mfma_f32_32x32x16_bf16 v[16:31], a[0:3], a[20:23], v[16:31]\n"
    "ds_read_b64_tr_b16 a[14:15], v108 offset:2816\n"
    "ds_read_b128 a[24:27], v101 offset:39872\n"
    "ds_read_b128 a[28:31], v105 offset:39872\n"
    "s_add_u32 m0, %[m0b], 0\n"
    "s_nop 0\n"
    "buffer_load_dwordx4 v109, %[srd], %[soff] offen lds\n"
    "v_mfma_f32_32x32x16_bf16 v[32:47], a[4:7], a[16:19], v[32:47]\n"
    "s_add_u32 m0, %[m0b], 1024\n"
    "s_add_u32 %[tmp], %[soff], 27648\n"
    "buffer_load_dwordx4 v109, %[srd], %[tmp] offen lds\n"
    "s_add_u32 %[soff], %[soff], 55296\n"
    "v_mfma_f32_32x32x16_bf16 v[48:63], a[4:7], a[20:23], v[48:63]\n"
    "s_waitcnt lgkmcnt(0)\n"
    "v_mfma_f32_32x32x16_bf16 v[0:15], a[8:11], a[24:27], v[0:15]\n"
    "s_waitcnt vmcnt(8)\n"
    "ds_read_b64_tr_b16 a[0:1], v108 offset:4096\n"
    "ds_read_b64_tr_b16 a[2:3], v108 offset:4608\n"
    "ds_read_b64_tr_b16 a[4:5], v108 offset:4352\n"
    "v_mfma_f32_32x32x16_bf16 v[16:31], a[8:11], a[28:31], v[16:31]\n"
    "ds_read_b64_tr_b16 a[6:7], v108 offset:4864\n"
    "ds_read_b128 a[16:19], v102 offset:40896\n"
    "ds_read_b128 a[20:23], v106 offset:40896\n"
    "s_add_u32 m0, %[m0b], 2048\n"
    "s_nop 0\n"
    "buffer_load_dwordx4 v109, %[srd], %[soff] offen lds\n"
    "v_mfma_f32_32x32x16_bf16 v[32:47], a[12:15], a[24:27], v[32:47]\n"
    "s_add_u32 m0, %[m0b], 3072\n"
    "s_add_u32 %[tmp], %[soff], 27648\n"
    "buffer_load_dwordx4 v109, %[srd], %[tmp] offen lds\n"
    "s_add_u32 %[soff], %[soff], 55296\n"
    "v_mfma_f32_32x32x16_bf16 v[48:63], a[12:15], a[28:31], v[48:63]\n"
    "s_waitcnt lgkmcnt(0)\n"
    "v_mfma_f32_32x32x16_bf16 v[0:15], a[0:3], a[16:19], v[0:15]\n"
    "s_waitcnt vmcnt(8)\n"
    "ds_read_b64_tr_b16 a[8:9], v108 offset:6144\n"
    "ds_read_b64_tr_b16 a[10:11], v108 offset:6656\n"
    "ds_read_b64_tr_b16 a[12:13], v108 offset:6400\n"
    "v_mfma_f32_32x32x16_bf16 v[16:31], a[0:3], a[20:23], v[16:31]\n"
    "ds_read_b64_tr_b16 a[14:15], v108 offset:6912\n"
    "ds_read_b128 a[24:27], v103 offset:41920\n"
    "ds_read_b128 a[28:31], v107 offset:41920\n"
    "s_add_u32 m0, %[m0b], 4096\n"
    "s_nop 0\n"
    "buffer_load_dwordx4 v109, %[srd], %[soff] offen lds\n"
    "v_mfma_f32_32x32x16_bf16 v[32:47], a[4:7], a[16:19], v[32:47]\n"
    "s_add_u32 m0, %[m0b], 5120\n"
    "s_add_u32 %[tmp], %[soff], 27648\n"
    "buffer_load_dwordx4 v109, %[srd], %[tmp] offen lds\n"
    "s_add_u32 %[soff], %[soff], 55296\n"
    "v_mfma_f32_32x32x16_bf16 v[48:63], a[4:7], a[20:23], v[48:63]\n"
    "s_waitcnt lgkmcnt(0)\n"
    "v_mfma_f32_32x32x16_bf16 v[0:15], a[8:11], a[24:27], v[0:15]\n"
    "s_waitcnt vmcnt(8)\n"
    "ds_read_b64_tr_b16 a[0:1], v108 offset:8192\n"
    "ds_read_b64_tr_b16 a[2:3], v108 offset:8704\n"
    "ds_read_b64_tr_b16 a[4:5], v108 offset:8448\n"
    "v_mfma_f32_32x32x16_bf16 v[16:31], a[8:11], a[28:31], v[16:31]\n"
    "ds_read_b64_tr_b16 a[6:7], v108 offset:8960\n"
    "ds_read_b128 a[16:19], v100 offset:42944\n"
    "ds_read_b128 a[20:23], v104 offset:42944\n"
    "s_add_u32 m0, %[m0b], 6144\n"
    "s_nop 0\n"
    "buffer_load_dwordx4 v109, %[srd], %[soff] offen lds\n"
    "v_mfma_f32_32x32x16_bf16 v[32:47], a[12:15], a[24:27], v[32:47]\n"
    "s_add_u32 m0, %[m0b], 7168\n"
    "s_add_u32 %[tmp], %[soff], 27648\n"
    "buffer_load_dwordx4 v109, %[srd], %[tmp] offen lds\n"
    "s_add_u32 %[soff], %[soff], 55296\n"
    "v_mfma_f32_32x32x16_bf16 v[48:63], a[12:15], a[28:31], v[48:63]\n"
    "s_waitcnt lgkmcnt(0)\n"
    "v_mfma_f32_32x32x16_bf16 v[0:15], a[0:3], a[16:19], v[0:15]\n"
    "s_waitcnt vmcnt(8)\n"
    "ds_read_b64_tr_b16 a[8:9], v108 offset:10240\n"
    "ds_read_b64_tr_b16 a[10:11], v108 offset:10752\n"
    "ds_read_b64_tr_b16 a[12:13], v108 offset:10496\n"
    "v_mfma_f32_32x32x16_bf16 v[16:31], a[0:3], a[20:23], v[16:31]\n"
    "ds_read_b64_tr_b16 a[14:15], v108 offset:11008\n"
    "ds_read_b128 a[24:27], v101 offset:43968\n"
    "ds_read_b128 a[28:31], v105 offset:43968\n"
    "s_add_u32 m0, %[m0b], 8192\n"
    "s_nop 0\n"
    "buffer_load_dwordx4 v109, %[srd], %[soff] offen lds\n"
    "v_mfma_f32_32x32x16_bf16 v[32:47], a[4:7], a[16:19], v[32:47]\n"
    "s_add_u32 m0, %[m0b], 9216\n"
    "s_add_u32 %[tmp], %[soff], 27648\n"
    "buffer_load_dwordx4 v109, %[srd], %[tmp] offen lds\n"
    "s_add_u32 %[soff], %[soff], 55296\n"
    "v_mfma_f32_32x32x16_bf16 v[48:63], a[4:7], a[20:23], v[48:63]\n"
    "s_waitcnt lgkmcnt(0)\n"
    "v_mfma_f32_32x32x16_bf16 v[0:15], a[8:11], a[24:27], v[0:15]\n"
    "s_waitcnt vmcnt(8)\n"
    "ds_read_b64_tr_b16 a[0:1], v108 offset:0\n"
    "ds_read_b64_tr_b16 a[2:3], v108 offset:512\n"
    "ds_read_b64_tr_b16 a[4:5], v108 offset:256\n"
    "v_mfma_f32_32x32x16_bf16 v[16:31], a[8:11], a[28:31], v[16:31]\n"
    "ds_read_b64_tr_b16 a[6:7], v108 offset:768\n"
    "ds_read_b128 a[16:19], v102 offset:44992\n"
    "ds_read_b128 a[20:23], v106 offset:44992\n"
    "s_add_u32 m0, %[m0b], 10240\n"
    "s_nop 0\n"
    "buffer_load_dwordx4 v109, %[srd], %[soff] offen lds\n"
    "v_mfma_f32_32x32x16_bf16 v[32:47], a[12:15], a[24:27], v[32:47]\n"
    "s_add_u32 m0, %[m0b], 11264\n"
    "s_add_u32 %[tmp], %[soff], 27648\n"
    "buffer_load_dwordx4 v109, %[srd], %[tmp] offen lds\n"
    "s_add_u32 %[soff], %[soff], 55296\n"
    "v_mfma_f32_32x32x16_bf16 v[48:63], a[12:15], a[28:31], v[48:63]\n"
    "s_waitcnt lgkmcnt(0)\n"
    "v_mfma_f32_32x32x16_bf16 v[0:15], a[0:3], a[16:19], v[0:15]\n"
    "s_waitcnt vmcnt(8)\n"
    "ds_read_b64_tr_b16 a[8:9], v108 offset:2048\n"
    "ds_read_b64_tr_b16 a[10:11], v108 offset:2560\n"
    "ds_read_b64_tr_b16 a[12:13], v108 offset:2304\n"
    "v_mfma_f32_32x32x16_bf16 v[16:31], a[0:3], a[20:23], v[16:31]\n"
    "ds_read_b64_tr_b16 a[14:15], v108 offset:2816\n"
    "ds_read_b128 a[24:27], v103 offset:46016\n"
    "ds_read_b128 a[28:31], v107 offset:46016\n"
    "s_add_u32 m0, %[m0b], 0\n"
    "s_nop 0\n"
    "buffer_load_dwordx4 v109, %[srd], %[soff] offen lds\n"
    "v_mfma_f32_32x32x16_bf16 v[32:47], a[4:7], a[16:19], v[32:47]\n"
    "s_add_u32 m0, %[m0b], 1024\n"
    "s_add_u32 %[tmp], %[soff], 27648\n"
    "buffer_load_dwordx4 v109, %[srd], %[tmp] offen lds\n"
    "s_add_u32 %[soff], %[soff], 55296\n"
    "v_mfma_f32_32x32x16_bf16 v[48:63], a[4:7], a[20:23], v[48:63]\n"
    "s_waitcnt lgkmcnt(0)\n"
    "v_mfma_f32_32x32x16_bf16 v[0:15], a[8:11], a[24:27], v[0:15]\n"
    "s_waitcnt vmcnt(8)\n"
    "ds_read_b64_tr_b16 a[0:1], v108 offset:4096\n"
    "ds_read_b64_tr_b16 a[2:3], v108 offset:4608\n"
    "ds_read_b64_tr_b16 a[4:5], v108 offset:4352\n"
    "v_mfma_f32_32x32x16_bf16 v[16:31], a[8:11], a[28:31], v[16:31]\n"
    "ds_read_b64_tr_b16 a[6:7], v108 offset:4864\n"
    "ds_read_b128 a[16:19], v100 offset:47040\n"
    "ds_read_b128 a[20:23], v104 offset:47040\n"
    "s_add_u32 m0, %[m0b], 2048\n"
    "s_nop 0\n"
    "buffer_load_dwordx4 v109, %[srd], %[soff] offen lds\n"
    "v_mfma_f32_32x32x16_bf16 v[32:47], a[12:15], a[24:27], v[32:47]\n"
    "s_add_u32 m0, %[m0b], 3072\n"
    "s_add_u32 %[tmp], %[soff], 27648\n"
    "buffer_load_dwordx4 v109, %[srd], %[tmp] offen lds\n"
    "s_add_u32 %[soff], %[soff], 55296\n"
    "v_mfma_f32_32x32x16_bf16 v[48:63], a[12:15], a[28:31], v[48:63]\n"
    "s_waitcnt lgkmcnt(0)\n"
    "v_mfma_f32_32x32x16_bf16 v[0:15], a[0:3], a[16:19], v[0:15]\n"
    "s_waitcnt vmcnt(8)\n"
    "ds_read_b64_tr_b16 a[8:9], v108 offset:6144\n"
    "ds_read_b64_tr_b16 a[10:11], v108 offset:6656\n"
    "ds_read_b64_tr_b16 a[12:13], v108 offset:6400\n"
    "v_mfma_f32_32x32x16_bf16 v[16:31], a[0:3], a[20:23], v[16:31]\n"
    "ds_read_b64_tr_b16 a[14:15], v108 offset:6912\n"
    "ds_read_b128 a[24:27], v101 offset:48064\n"
    "ds_read_b128 a[28:31], v105 offset:48064\n"
    "s_add_u32 m0, %[m0b], 4096\n"
    "s_nop 0\n"
    "buffer_load_dwordx4 v109, %[srd], %[soff] offen lds\n"
    "v_mfma_f32_32x32x16_bf16 v[32:47], a[4:7], a[16:19], v[32:47]\n"
    "s_add_u32 m0, %[m0b], 5120\n"
    "s_add_u32 %[tmp], %[soff], 27648\n"
    "buffer_load_dwordx4 v109, %[srd], %[tmp] offen lds\n"
    "s_add_u32 %[soff], %[soff], 55296\n"
    "v_mfma_f32_32x32x16_bf16 v[48:63], a[4:7], a[20:23], v[48:63]\n"
    "s_waitcnt lgkmcnt(0)\n"
    "v_mfma_f32_32x32x16_bf16 v[0:15], a[8:11], a[24:27], v[0:15]\n"
    "s_waitcnt vmcnt(8)\n"
    "ds_read_b64_tr_b16 a[0:1], v108 offset:8192\n"
    "ds_read_b64_tr_b16 a[2:3], v108 offset:8704\n"
    "ds_read_b64_tr_b16 a[4:5], v108 offset:8448\n"
    "v_mfma_f32_32x32x16_bf16 v[16:31], a[8:11], a[28:31], v[16:31]\n"
    "ds_read_b64_tr_b16 a[6:7], v108 offset:8960\n"
    "ds_read_b128 a[16:19], v102 offset:49088\n"
    "ds_read_b128 a[20:23], v106 offset:49088\n"
    "s_add_u32 m0, %[m0b], 6144\n"
    "s_nop 0\n"
    "buffer_load_dwordx4 v109, %[srd], %[soff] offen lds\n"
    "v_mfma_f32_32x32x16_bf16 v[32:47], a[12:15], a[24:27], v[32:47]\n"
    "s_add_u32 m0, %[m0b], 7168\n"
    "s_add_u32 %[tmp], %[soff], 27648\n"
    "buffer_load_dwordx4 v109, %[srd], %[tmp] offen lds\n"
    "s_add_u32 %[soff], %[soff], 55296\n"
    "v_mfma_f32_32x32x16_bf16 v[48:63], a[12:15], a[28:31], v[48:63]\n"
    "s_waitcnt lgkmcnt(0)\n"
    "v_mfma_f32_32x32x16_bf16 v[0:15], a[0:3], a[16:19], v[0:15]\n"
    "s_waitcnt vmcnt(8)\n"
    "ds_read_b64_tr_b16 a[8:9], v108 offset:10240\n"
    "ds_read_b64_tr_b16 a[10:11], v108 offset:10752\n"
    "ds_read_b64_tr_b16 a[12:13], v108 offset:10496\n"
    "v_mfma_f32_32x32x16_bf16 v[16:31], a[0:3], a[20:23], v[16:31]\n"
    "ds_read_b64_tr_b16 a[14:15], v108 offset:11008\n"
    "ds_read_b128 a[24:27], v103 offset:50112\n"
    "ds_read_b128 a[28:31], v107 offset:50112\n"
    "v_mfma_f32_32x32x16_bf16 v[32:47], a[4:7], a[16:19], v[32:47]\n"
    "v_mfma_f32_32x32x16_bf16 v[48:63], a[4:7], a[20:23], v[48:63]\n"
    "s_waitcnt lgkmcnt(0)\n"
    "v_mfma_f32_32x32x16_bf16 v[0:15], a[8:11], a[24:27], v[0:15]\n"
    "s_waitcnt vmcnt(6)\n"
    "ds_read_b64_tr_b16 a[0:1], v108 offset:0\n"
    "ds_read_b64_tr_b16 a[2:3], v108 offset:512\n"
    "ds_read_b64_tr_b16 a[4:5], v108 offset:256\n"
    "v_mfma_f32_32x32x16_bf16 v[16:31], a[8:11], a[28:31], v[16:31]\n"
    "ds_read_b64_tr_b16 a[6:7], v108 offset:768\n"
    "ds_read_b128 a[16:19], v100 offset:51136\n"
    "ds_read_b128 a[20:23], v104 offset:51136\n"
    "v_mfma_f32_32x32x16_bf16 v[32:47], a[12:15], a[24:27], v[32:47]\n"
    "v_mfma_f32_32x32x16_bf16 v[48:63], a[12:15], a[28:31], v[48:63]\n"
    "s_waitcnt lgkmcnt(0)\n"
    "v_mfma_f32_32x32x16_bf16 v[0:15], a[0:3], a[16:19], v[0:15]\n"
    "s_waitcnt vmcnt(4)\n"
    "ds_read_b64_tr_b16 a[8:9], v108 offset:2048\n"
    "ds_read_b64_tr_b16 a[10:11], v108 offset:2560\n"
    "ds_read_b64_tr_b16 a[12:13], v108 offset:2304\n"
    "v_mfma_f32_32x32x16_bf16 v[16:31], a[0:3], a[20:23], v[16:31]\n"
    "ds_read_b64_tr_b16 a[14:15], v108 offset:2816\n"
    "ds_read_b128 a[24:27], v101 offset:52160\n"
    "ds_read_b128 a[28:31], v105 offset:52160\n"
    "v_mfma_f32_32x32x16_bf16 v[32:47], a[4:7], a[16:19], v[32:47]\n"
    "v_mfma_f32_32x32x16_bf16 v[48:63], a[4:7], a[20:23], v[48:63]\n"
    "s_waitcnt lgkmcnt(0)\n"
    "v_mfma_f32_32x32x16_bf16 v[0:15], a[8:11], a[24:27], v[0:15]\n"
    "s_waitcnt vmcnt(2)\n"
    "ds_read_b64_tr_b16 a[0:1], v108 offset:4096\n"
    "ds_read_b64_tr_b16 a[2:3], v108 offset:4608\n"
    "ds_read_b64_tr_b16 a[4:5], v108 offset:4352\n"
    "v_mfma_f32_32x32x16_bf16 v[16:31], a[8:11], a[28:31], v[16:31]\n"
    "ds_read_b64_tr_b16 a[6:7], v108 offset:4864\n"
    "ds_read_b128 a[16:19], v102 offset:53184\n"
    "ds_read_b128 a[20:23], v106 offset:53184\n"
    "v_mfma_f32_32x32x16_bf16 v[32:47], a[12:15], a[24:27], v[32:47]\n"
    "v_mfma_f32_32x32x16_bf16 v[48:63], a[12:15], a[28:31], v[48:63]\n"
    "s_waitcnt lgkmcnt(0)\n"
    "v_mfma_f32_32x32x16_bf16 v[0:15], a[0:3], a[16:19], v[0:15]\n"
    "s_waitcnt vmcnt(0)\n"
    "ds_read_b64_tr_b16 a[8:9], v108 offset:6144\n"
    "ds_read_b64_tr_b16 a[10:11], v108 offset:6656\n"
    "ds_read_b64_tr_b16 a[12:13], v108 offset:6400\n"
    "v_mfma_f32_32x32x16_bf16 v[16:31], a[0:3], a[20:23], v[16:31]\n"
    "ds_read_b64_tr_b16 a[14:15], v108 offset:6912\n"
    "ds_read_b128 a[24:27], v103 offset:54208\n"
    "ds_read_b128 a[28:31], v107 offset:54208\n"
    "v_mfma_f32_32x32x16_bf16 v[32:47], a[4:7], a[16:19], v[32:47]\n"
    "v_mfma_f32_32x32x16_bf16 v[48:63], a[4:7], a[20:23], v[48:63]\n"
    "s_waitcnt lgkmcnt(0)\n"
    "v_mfma_f32_32x32x16_bf16 v[0:15], a[8:11], a[24:27], v[0:15]\n"
    "v_mfma_f32_32x32x16_bf16 v[16:31], a[8:11], a[28:31], v[16:31]\n"
    "v_mfma_f32_32x32x16_bf16 v[32:47], a[12:15], a[24:27], v[32:47]\n"
    "v_mfma_f32_32x32x16_bf16 v[48:63], a[12:15], a[28:31], v[48:63]\n"
    "s_mov_b32 m0, %[keep]\n"
    "s_setprio 3\n"
    "s_nop 7\n"
    "v_cvt_pk_bf16_f32 v0, v0, v1\n"
    "v_cvt_pk_bf16_f32 v1, v2, v3\n"
    "v_cvt_pk_bf16_f32 v2, v4, v5\n"
    "v_cvt_pk_bf16_f32 v3, v6, v7\n"
    "v_pk_max_i16 v0, v0, 0\n"
    "v_pk_max_i16 v1, v1, 0\n"
    "v_pk_max_i16 v2, v2, 0\n"
    "v_pk_max_i16 v3, v3, 0\n"
    "v_cvt_pk_bf16_f32 v8, v8, v9\n"
    "v_cvt_pk_bf16_f32 v9, v10, v11\n"
    "v_cvt_pk_bf16_f32 v10, v12, v13\n"
    "v_cvt_pk_bf16_f32 v11, v14, v15\n"
    "v_pk_max_i16 v8, v8, 0\n"
    "v_pk_max_i16 v9, v9, 0\n"
    "v_pk_max_i16 v10, v10, 0\n"
    "v_pk_max_i16 v11, v11, 0\n"
    "v_cvt_pk_bf16_f32 v16, v16, v17\n"
    "v_cvt_pk_bf16_f32 v17, v18, v19\n"
    "v_cvt_pk_bf16_f32 v18, v20, v21\n"
    "v_cvt_pk_bf16_f32 v19, v22, v23\n"
    "v_pk_max_i16 v16, v16, 0\n"
    "v_pk_max_i16 v17, v17, 0\n"
    "v_pk_max_i16 v18, v18, 0\n"
    "v_pk_max_i16 v19, v19, 0\n"
    "v_cvt_pk_bf16_f32 v24, v24, v25\n"
    "v_cvt_pk_bf16_f32 v25, v26, v27\n"
    "v_cvt_pk_bf16_f32 v26, v28, v29\n"
    "v_cvt_pk_bf16_f32 v27, v30, v31\n"
    "v_pk_max_i16 v24, v24, 0\n"
    "v_pk_max_i16 v25, v25, 0\n"
    "v_pk_max_i16 v26, v26, 0\n"
    "v_pk_max_i16 v27, v27, 0\n"
    "v_cvt_pk_bf16_f32 v32, v32, v33\n"
    "v_cvt_pk_bf16_f32 v33, v34, v35\n"
    "v_cvt_pk_bf16_f32 v34, v36, v37\n"
    "v_cvt_pk_bf16_f32 v35, v38, v39\n"
    "v_pk_max_i16 v32, v32, 0\n"
    "v_pk_max_i16 v33, v33, 0\n"
    "v_pk_max_i16 v34, v34, 0\n"
    "v_pk_max_i16 v35, v35, 0\n"
    "v_cvt_pk_bf16_f32 v40, v40, v41\n"
    "v_cvt_pk_bf16_f32 v41, v42, v43\n"
    "v_cvt_pk_bf16_f32 v42, v44, v45\n"
    "v_cvt_pk_bf16_f32 v43, v46, v47\n"
    "v_pk_max_i16 v40, v40, 0\n"
    "v_pk_max_i16 v41, v41, 0\n"
    "v_pk_max_i16 v42, v42, 0\n"
    "v_pk_max_i16 v43, v43, 0\n"
    "v_cvt_pk_bf16_f32 v48, v48, v49\n"
    "v_cvt_pk_bf16_f32 v49, v50, v51\n"
    "v_cvt_pk_bf16_f32 v50, v52, v53\n"
    "v_cvt_pk_bf16_f32 v51, v54, v55\n"
    "v_pk_max_i16 v48, v48, 0\n"
    "v_pk_max_i16 v49, v49, 0\n"
    "v_pk_max_i16 v50, v50, 0\n"
    "v_pk_max_i16 v51, v51, 0\n"
    "v_cvt_pk_bf16_f32 v56, v56, v57\n"
    "v_cvt_pk_bf16_f32 v57, v58, v59\n"
    "v_cvt_pk_bf16_f32 v58, v60, v61\n"
    "v_cvt_pk_bf16_f32 v59, v62, v63\n"
    "v_pk_max_i16 v56, v56, 0\n"
    "v_pk_max_i16 v57, v57, 0\n"
    "v_pk_max_i16 v58, v58, 0\n"
    "v_pk_max_i16 v59, v59, 0\n"
    "v_mov_b32 v4, -1\n"
    "v_mov_b32 v5, -1\n"
    "v_mov_b32 v6, -1\n"
    "v_mov_b32 v7, -1\n"
    "s_setprio 0\n"
    "global_store_dwordx4 v110, v[0:3], %[hb] offset:-4096 sc1\n"
    "global_store_dwordx4 v110, v[8:11], %[hb] offset:-2048 sc1\n"
    "global_store_dwordx4 v110, v[16:19], %[hb] offset:-3072 sc1\n"
    "global_store_dwordx4 v110, v[24:27], %[hb] offset:-1024 sc1\n"
    "global_store_dwordx4 v110, v[32:35], %[hb] offset:0 sc1\n"
    "global_store_dwordx4 v110, v[40:43], %[hb] offset:2048 sc1\n"
    "global_store_dwordx4 v110, v[48:51], %[hb] offset:1024 sc1\n"
    "global_store_dwordx4 v110, v[56:59], %[hb] offset:3072 sc1\n"
    "s_cmp_eq_u32 %[q], 3\n"
    "s_cbranch_scc1 FP%=\n"
    "global_store_dwordx4 v110, v[4:7], %[pz] offset:0 sc1\n"
    "global_store_dwordx4 v110, v[4:7], %[pz] offset:1024 sc1\n"
    "global_store_dwordx4 v110, v[4:7], %[pz] offset:2048 sc1\n"
    "global_store_dwordx4 v110, v[4:7], %[pz] offset:3072 sc1\n"
    "FP%=:\n"
    "s_waitcnt vmcnt(0)\n"
    "s_setprio 3\n"
        : [keep] "=&s"(keep), [tmp] "=&s"(tmp), [soff] "+s"(soff)
        : "{v[100:107]}"(xbv), "{v[108:111]}"(f108), "{v[112:119]}"(g112), "{v[120:123]}"(g120),
          [srd] "s"(srd1), [m0b] "s"(rfl(m0b + 20480u)), [hb] "s"(ws + W_H + (size_t)g * 221184 + un * 8192 + 4096),
          [q] "s"(q), [kvh] "s"(ws + W_KV + (q & 1) * 32 * KVT), [pz] "s"(ws + W_O + (size_t)g * 81920 + c * 10240 + q * 3072)
        : "v0", "v1", "v2", "v3", "v4", "v5", "v6", "v7", "v8", "v9", "v10", "v11", "v12", "v13", "v14", "v15", "v16", "v17", "v18", "v19", "v20", "v21", "v22", "v23", "v24", "v25", "v26", "v27", "v28", "v29", "v30", "v31", "v32", "v33", "v34", "v35", "v36", "v37", "v38", "v39", "v40", "v41", "v42", "v43", "v44", "v45", "v46", "v47", "v48", "v49", "v50", "v51", "v52", "v53", "v54", "v55", "v56", "v57", "v58", "v59", "v60", "v61", "v62", "v63", "v64", "v65", "a0", "a1", "a2", "a3", "a4", "a5", "a6", "a7", "a8", "a9", "a10", "a11", "a12", "a13", "a14", "a15", "a16", "a17", "a18", "a19", "a20", "a21", "a22", "a23", "a24", "a25", "a26", "a27", "a28", "a29", "a30", "a31", "scc", "memory");
      if (lane < 2) st8(ws + W_HF + lane * 8192 + g * 256 + un * 8, epoch);
    } else {
      const u32 p = g * 5 + (c < 6 ? c >> 1 : c - 3);
#pragma unroll 1
      for (u32 rd = 0; rd < 2; ++rd) {
        char* dst[5]; u32 n = 0;
#pragma unroll
        for (u32 i = 0; i < 5; ++i) {
          const u32 ux = p + 160u * (rd * 5 + i), u = ux < 1480u ? ux : p, s = u / 10u, cp = u % 10u, m = s - 108u, sub = m % 5u;
          const bool isw2 = s < 108u, left = !isw2 && sub == 4u;
          n += ux < 1480u;
          dst[i] = ws + W_WF + s * 20480u + cp * 2048u;
          asm volatile(
    "s_setprio 0\n"
    "s_mov_b32 s64, m0\n"
    "s_mov_b32 m0, %[m0s]\n"
    "s_add_u32 s63, %[so], %[dl]\n"
    "buffer_load_dwordx4 %[vo], %[srd], %[so] offen lds\n"
    "s_add_u32 m0, %[m0s], 1024\n"
    "s_nop 0\n"
    "buffer_load_dwordx4 %[vo], %[srd], s63 offen lds\n"
    "s_mov_b32 m0, s64\n"
    "s_setprio 3\n"
            :: [vo] "v"(((left ? 20u : 4u) * hi + q4) * 1280u + (u32)(g4 & 1) * 64u + p4 * 16u), [srd] "s"(isw2 ? mksrd(w2, 2211840) : mksrd(wo, 819200)),
               [so] "s"((isw2 ? 16u * s : 80u * (m / 5u) + (left ? 16u : 20u * sub)) * 1280u + cp * 128u), [dl] "s"((left ? 40u : 8u) * 1280u), [m0s] "s"(m0b + 20480u + i * 2048u)
            : "s63", "s64", "scc", "memory");
        }
        asm volatile(
    "s_setprio 0\n"
    "s_waitcnt vmcnt(0)\n"
    "ds_read_b64_tr_b16 v[0:1], %[tr] offset:0\n"
    "ds_read_b64_tr_b16 v[2:3], %[tr] offset:1024\n"
    "ds_read_b64_tr_b16 v[4:5], %[tr] offset:256\n"
    "ds_read_b64_tr_b16 v[6:7], %[tr] offset:1280\n"
    "ds_read_b64_tr_b16 v[8:9], %[tr] offset:2048\n"
    "ds_read_b64_tr_b16 v[10:11], %[tr] offset:3072\n"
    "ds_read_b64_tr_b16 v[12:13], %[tr] offset:2304\n"
    "ds_read_b64_tr_b16 v[14:15], %[tr] offset:3328\n"
    "ds_read_b64_tr_b16 v[16:17], %[tr] offset:4096\n"
    "ds_read_b64_tr_b16 v[18:19], %[tr] offset:5120\n"
    "ds_read_b64_tr_b16 v[20:21], %[tr] offset:4352\n"
    "ds_read_b64_tr_b16 v[22:23], %[tr] offset:5376\n"
    "ds_read_b64_tr_b16 v[24:25], %[tr] offset:6144\n"
    "ds_read_b64_tr_b16 v[26:27], %[tr] offset:7168\n"
    "ds_read_b64_tr_b16 v[28:29], %[tr] offset:6400\n"
    "ds_read_b64_tr_b16 v[30:31], %[tr] offset:7424\n"
    "ds_read_b64_tr_b16 v[32:33], %[tr] offset:8192\n"
    "ds_read_b64_tr_b16 v[34:35], %[tr] offset:9216\n"
    "ds_read_b64_tr_b16 v[36:37], %[tr] offset:8448\n"
    "ds_read_b64_tr_b16 v[38:39], %[tr] offset:9472\n"
    "s_waitcnt lgkmcnt(15)\n"
    "s_cmp_le_u32 %[n], 0\n"
    "s_cbranch_scc1 RB%=\n"
    "global_store_dwordx4 %[l16], v[0:3], %[d0] sc1\n"
    "global_store_dwordx4 %[l16], v[4:7], %[d0] offset:1024 sc1\n"
    "s_waitcnt lgkmcnt(12)\n"
    "s_cmp_le_u32 %[n], 1\n"
    "s_cbranch_scc1 RB%=\n"
    "global_store_dwordx4 %[l16], v[8:11], %[d1] sc1\n"
    "global_store_dwordx4 %[l16], v[12:15], %[d1] offset:1024 sc1\n"
    "s_waitcnt lgkmcnt(8)\n"
    "s_cmp_le_u32 %[n], 2\n"
    "s_cbranch_scc1 RB%=\n"
    "global_store_dwordx4 %[l16], v[16:19], %[d2] sc1\n"
    "global_store_dwordx4 %[l16], v[20:23], %[d2] offset:1024 sc1\n"
    "s_waitcnt lgkmcnt(4)\n"
    "s_cmp_le_u32 %[n], 3\n"
    "s_cbranch_scc1 RB%=\n"
    "global_store_dwordx4 %[l16], v[24:27], %[d3] sc1\n"
    "global_store_dwordx4 %[l16], v[28:31], %[d3] offset:1024 sc1\n"
    "s_waitcnt lgkmcnt(0)\n"
    "s_cmp_le_u32 %[n], 4\n"
    "s_cbranch_scc1 RB%=\n"
    "global_store_dwordx4 %[l16], v[32:35], %[d4] sc1\n"
    "global_store_dwordx4 %[l16], v[36:39], %[d4] offset:1024 sc1\n"
    "RB%=:\n"
    "s_waitcnt lgkmcnt(0)\n"
    "s_setprio 3\n"
          :: [tr] "v"(m0b + 20480u + hi * 512 + trl), [l16] "v"(f108[2]), [d0] "s"(dst[0]), [d1] "s"(dst[1]), [d2] "s"(dst[2]), [d3] "s"(dst[3]), [d4] "s"(dst[4]), [n] "s"(rfl(n))
          : "v0", "v1", "v2", "v3", "v4", "v5", "v6", "v7", "v8", "v9", "v10", "v11", "v12", "v13", "v14", "v15", "v16", "v17", "v18", "v19", "v20", "v21", "v22", "v23", "v24", "v25", "v26", "v27", "v28", "v29", "v30", "v31", "v32", "v33", "v34", "v35", "v36", "v37", "v38", "v39", "scc", "memory");
      }
      drain();
      if (lane < 8) st8(ws + W_WFL + lane * 2048 + p * 8, epoch);
    }
    if (lane == 0) lds_add(lds + L_FL + 4);
    if (q < 2) {
      poll2(ws + W_WFL + (b & 7) * 2048, q ? 1024 + (lane & 15) * 16 : lane * 16, epoch);
      if (lane == 0) lds_add(lds + L_FL + 56);
    }
    lds_wait<4>(lds + L_FL + 56, 2u);
    const u32 own = rfl(pp == q ? 1u : 0u), sw = own & ty;
    const char* obA = ws + W_O + (size_t)g * 81920 + q * 20480 + sw * 10240 + 4096; const char* obB = obA + (sw ? -10240 : 10240);
    asm volatile(
    "s_setprio 0\n"
    "s_mov_b64 s[60:61], %[wob]\n"
    "global_load_dwordx4 a[80:83], v112, s[60:61] sc1\n"
    "global_load_dwordx4 a[84:87], v113, s[60:61] sc1\n"
    "global_load_dwordx4 a[88:91], v114, s[60:61] sc1\n"
    "s_add_u32 s60, s60, 20480\n"
    "s_addc_u32 s61, s61, 0\n"
    "global_load_dwordx4 a[92:95], v112, s[60:61] sc1\n"
    "global_load_dwordx4 a[96:99], v113, s[60:61] sc1\n"
    "global_load_dwordx4 a[100:103], v114, s[60:61] sc1\n"
    "s_add_u32 s60, s60, 20480\n"
    "s_addc_u32 s61, s61, 0\n"
    "global_load_dwordx4 a[104:107], v112, s[60:61] sc1\n"
    "global_load_dwordx4 a[108:111], v113, s[60:61] sc1\n"
    "global_load_dwordx4 a[112:115], v114, s[60:61] sc1\n"
    "s_add_u32 s60, s60, 20480\n"
    "s_addc_u32 s61, s61, 0\n"
    "global_load_dwordx4 a[116:119], v112, s[60:61] sc1\n"
    "global_load_dwordx4 a[120:123], v113, s[60:61] sc1\n"
    "global_load_dwordx4 a[124:127], v114, s[60:61] sc1\n"
    "s_add_u32 s60, s60, 20480\n"
    "s_addc_u32 s61, s61, 0\n"
    "global_load_dwordx4 v[0:3], v112, s[60:61] sc1\n"
    "global_load_dwordx4 v[4:7], v113, s[60:61] sc1\n"
    "global_load_dwordx4 v[8:11], v114, s[60:61] sc1\n"
    "s_add_u32 s60, s60, 20480\n"
    "s_addc_u32 s61, s61, 0\n"
    "s_ashr_i32 s63, %[wd], 31\n"
    "s_add_u32 s60, s60, %[wd]\n"
    "s_addc_u32 s61, s61, s63\n"
    "global_load_dwordx4 v[12:15], v112, s[60:61] sc1\n"
    "global_load_dwordx4 v[16:19], v113, s[60:61] sc1\n"
    "global_load_dwordx4 v[20:23], v114, s[60:61] sc1\n"
    "s_add_u32 s60, s60, 20480\n"
    "s_addc_u32 s61, s61, 0\n"
    "global_load_dwordx4 v[24:27], v112, s[60:61] sc1\n"
    "global_load_dwordx4 v[28:31], v113, s[60:61] sc1\n"
    "global_load_dwordx4 v[32:35], v114, s[60:61] sc1\n"
    "s_add_u32 s60, s60, 20480\n"
    "s_addc_u32 s61, s61, 0\n"
    "global_load_dwordx4 v[36:39], v112, s[60:61] sc1\n"
    "global_load_dwordx4 v[40:43], v113, s[60:61] sc1\n"
    "global_load_dwordx4 v[44:47], v114, s[60:61] sc1\n"
    "s_add_u32 s60, s60, 20480\n"
    "s_addc_u32 s61, s61, 0\n"
    "s_setprio 3\n"
    "s_mov_b32 s62, 0x10000\n"
    "G2H%=:\n"
    "global_load_dwordx2 v[72:73], v123, %[hfb] sc1\n"
    "s_waitcnt vmcnt(0)\n"
    "v_cmp_eq_u64 vcc, %[ep], v[72:73]\n"
    "s_nop 3\n"
    "s_cmp_eq_u64 vcc, exec\n"
    "s_cbranch_scc1 G2I%=\n"
    "s_sleep 2\n"
    "s_sub_u32 s62, s62, 1\n"
    "s_cmp_lg_u32 s62, 0\n"
    "s_cbranch_scc1 G2H%=\n"
    "G2I%=:\n"
    "s_setprio 0\n"
    "s_mov_b64 s[56:57], s[60:61]\n"
    "s_mov_b64 s[58:59], %[ab]\n"
    "s_mov_b64 s[60:61], %[wb]\n"
    "global_load_dwordx4 v[52:55], v112, s[60:61] sc1\n"
    "global_load_dwordx4 v[56:59], v113, s[60:61] sc1\n"
    "global_load_dwordx4 v[60:63], v114, s[60:61] sc1\n"
    "s_add_u32 s60, s60, 20480\n"
    "s_addc_u32 s61, s61, 0\n"
    "global_load_dwordx4 v[64:67], v115, s[58:59] offset:0 sc1\n"
    "global_load_dwordx4 v[68:71], v116, s[58:59] offset:0 sc1\n"
    "global_load_dwordx4 v[72:75], v112, s[60:61] sc1\n"
    "global_load_dwordx4 v[76:79], v113, s[60:61] sc1\n"
    "global_load_dwordx4 v[80:83], v114, s[60:61] sc1\n"
    "s_add_u32 s60, s60, 20480\n"
    "s_addc_u32 s61, s61, 0\n"
    "global_load_dwordx4 v[84:87], v115, s[58:59] offset:2048 sc1\n"
    "global_load_dwordx4 v[88:91], v116, s[58:59] offset:2048 sc1\n"
    "s_add_u32 s58, s58, 4096\n"
    "s_addc_u32 s59, s59, 0\n"
    "s_waitcnt vmcnt(5)\n"
    "v_mfma_f32_32x32x16_bf16 a[0:15], v[52:55], v[64:67], 0\n"
    "v_mfma_f32_32x32x16_bf16 a[16:31], v[52:55], v[68:71], 0\n"
    "global_load_dwordx4 v[52:55], v112, s[60:61] sc1\n"
    "v_mfma_f32_32x32x16_bf16 a[32:47], v[56:59], v[64:67], 0\n"
    "v_mfma_f32_32x32x16_bf16 a[48:63], v[56:59], v[68:71], 0\n"
    "global_load_dwordx4 v[56:59], v113, s[60:61] sc1\n"
    "v_mfma_f32_32x32x16_bf16 a[64:79], v[60:63], v[64:67], 0\n"
    "global_load_dwordx4 v[60:63], v114, s[60:61] sc1\n"
    "s_add_u32 s60, s60, 20480\n"
    "s_addc_u32 s61, s61, 0\n"
    "global_load_dwordx4 v[64:67], v115, s[58:59] offset:0 sc1\n"
    "global_load_dwordx4 v[68:71], v116, s[58:59] offset:0 sc1\n"
    "s_waitcnt vmcnt(5)\n"
    "v_mfma_f32_32x32x16_bf16 a[0:15], v[72:75], v[84:87], a[0:15]\n"
    "v_mfma_f32_32x32x16_bf16 a[16:31], v[72:75], v[88:91], a[16:31]\n"
    "global_load_dwordx4 v[72:75], v112, s[60:61] sc1\n"
    "v_mfma_f32_32x32x16_bf16 a[32:47], v[76:79], v[84:87], a[32:47]\n"
    "v_mfma_f32_32x32x16_bf16 a[48:63], v[76:79], v[88:91], a[48:63]\n"
    "global_load_dwordx4 v[76:79], v113, s[60:61] sc1\n"
    "v_mfma_f32_32x32x16_bf16 a[64:79], v[80:83], v[84:87], a[64:79]\n"
    "global_load_dwordx4 v[80:83], v114, s[60:61] sc1\n"
    "s_add_u32 s60, s60, 20480\n"
    "s_addc_u32 s61, s61, 0\n"
    "global_load_dwordx4 v[84:87], v115, s[58:59] offset:2048 sc1\n"
    "global_load_dwordx4 v[88:91], v116, s[58:59] offset:2048 sc1\n"
    "s_add_u32 s58, s58, 4096\n"
    "s_addc_u32 s59, s59, 0\n"
    "s_waitcnt vmcnt(5)\n"
    "v_mfma_f32_32x32x16_bf16 a[0:15], v[52:55], v[64:67], a[0:15]\n"
    "v_mfma_f32_32x32x16_bf16 a[16:31], v[52:55], v[68:71], a[16:31]\n"
    "global_load_dwordx4 v[52:55], v112, s[60:61] sc1\n"
    "v_mfma_f32_32x32x16_bf16 a[32:47], v[56:59], v[64:67], a[32:47]\n"
    "v_mfma_f32_32x32x16_bf16 a[48:63], v[56:59], v[68:71], a[48:63]\n"
    "global_load_dwordx4 v[56:59], v113, s[60:61] sc1\n"
    "v_mfma_f32_32x32x16_bf16 a[64:79], v[60:63], v[64:67], a[64:79]\n"
    "global_load_dwordx4 v[60:63], v114, s[60:61] sc1\n"
    "s_add_u32 s60, s60, 20480\n"
    "s_addc_u32 s61, s61, 0\n"
    "global_load_dwordx4 v[64:67], v115, s[58:59] offset:0 sc1\n"
    "global_load_dwordx4 v[68:71], v116, s[58:59] offset:0 sc1\n"
    "s_waitcnt vmcnt(5)\n"
    "v_mfma_f32_32x32x16_bf16 a[0:15], v[72:75], v[84:87], a[0:15]\n"
    "v_mfma_f32_32x32x16_bf16 a[16:31], v[72:75], v[88:91], a[16:31]\n"
    "global_load_dwordx4 v[72:75], v112, s[60:61] sc1\n"
    "v_mfma_f32_32x32x16_bf16 a[32:47], v[76:79], v[84:87], a[32:47]\n"
    "v_mfma_f32_32x32x16_bf16 a[48:63], v[76:79], v[88:91], a[48:63]\n"
    "global_load_dwordx4 v[76:79], v113, s[60:61] sc1\n"
    "v_mfma_f32_32x32x16_bf16 a[64:79], v[80:83], v[84:87], a[64:79]\n"
    "global_load_dwordx4 v[80:83], v114, s[60:61] sc1\n"
    "s_add_u32 s60, s60, 20480\n"
    "s_addc_u32 s61, s61, 0\n"
    "global_load_dwordx4 v[84:87], v115, s[58:59] offset:2048 sc1\n"
    "global_load_dwordx4 v[88:91], v116, s[58:59] offset:2048 sc1\n"
    "s_add_u32 s58, s58, 4096\n"
    "s_addc_u32 s59, s59, 0\n"
    "s_waitcnt vmcnt(5)\n"
    "v_mfma_f32_32x32x16_bf16 a[0:15], v[52:55], v[64:67], a[0:15]\n"
    "v_mfma_f32_32x32x16_bf16 a[16:31], v[52:55], v[68:71], a[16:31]\n"
    "global_load_dwordx4 v[52:55], v112, s[60:61] sc1\n"
    "v_mfma_f32_32x32x16_bf16 a[32:47], v[56:59], v[64:67], a[32:47]\n"
    "v_mfma_f32_32x32x16_bf16 a[48:63], v[56:59], v[68:71], a[48:63]\n"
    "global_load_dwordx4 v[56:59], v113, s[60:61] sc1\n"
    "v_mfma_f32_32x32x16_bf16 a[64:79], v[60:63], v[64:67], a[64:79]\n"
    "global_load_dwordx4 v[60:63], v114, s[60:61] sc1\n"
    "s_add_u32 s60, s60, 20480\n"
    "s_addc_u32 s61, s61, 0\n"
    "global_load_dwordx4 v[64:67], v115, s[58:59] offset:0 sc1\n"
    "global_load_dwordx4 v[68:71], v116, s[58:59] offset:0 sc1\n"
    "s_waitcnt vmcnt(5)\n"
    "v_mfma_f32_32x32x16_bf16 a[0:15], v[72:75], v[84:87], a[0:15]\n"
    "v_mfma_f32_32x32x16_bf16 a[16:31], v[72:75], v[88:91], a[16:31]\n"
    "global_load_dwordx4 v[72:75], v112, s[60:61] sc1\n"
    "v_mfma_f32_32x32x16_bf16 a[32:47], v[76:79], v[84:87], a[32:47]\n"
    "v_mfma_f32_32x32x16_bf16 a[48:63], v[76:79], v[88:91], a[48:63]\n"
    "global_load_dwordx4 v[76:79], v113, s[60:61] sc1\n"
    "v_mfma_f32_32x32x16_bf16 a[64:79], v[80:83], v[84:87], a[64:79]\n"
    "global_load_dwordx4 v[80:83], v114, s[60:61] sc1\n"
    "s_add_u32 s60, s60, 20480\n"
    "s_addc_u32 s61, s61, 0\n"
    "global_load_dwordx4 v[84:87], v115, s[58:59] offset:2048 sc1\n"
    "global_load_dwordx4 v[88:91], v116, s[58:59] offset:2048 sc1\n"
    "s_add_u32 s58, s58, 4096\n"
    "s_addc_u32 s59, s59, 0\n"
    "s_waitcnt vmcnt(5)\n"
    "v_mfma_f32_32x32x16_bf16 a[0:15], v[52:55], v[64:67], a[0:15]\n"
    "v_mfma_f32_32x32x16_bf16 a[16:31], v[52:55], v[68:71], a[16:31]\n"
    "global_load_dwordx4 v[52:55], v112, s[60:61] sc1\n"
    "v_mfma_f32_32x32x16_bf16 a[32:47], v[56:59], v[64:67], a[32:47]\n"
    "v_mfma_f32_32x32x16_bf16 a[48:63], v[56:59], v[68:71], a[48:63]\n"
    "global_load_dwordx4 v[56:59], v113, s[60:61] sc1\n"
    "v_mfma_f32_32x32x16_bf16 a[64:79], v[60:63], v[64:67], a[64:79]\n"
    "global_load_dwordx4 v[60:63], v114, s[60:61] sc1\n"
    "s_add_u32 s60, s60, 20480\n"
    "s_addc_u32 s61, s61, 0\n"
    "global_load_dwordx4 v[64:67], v115, s[58:59] offset:0 sc1\n"
    "global_load_dwordx4 v[68:71], v116, s[58:59] offset:0 sc1\n"
    "s_waitcnt vmcnt(5)\n"
    "v_mfma_f32_32x32x16_bf16 a[0:15], v[72:75], v[84:87], a[0:15]\n"
    "v_mfma_f32_32x32x16_bf16 a[16:31], v[72:75], v[88:91], a[16:31]\n"
    "global_load_dwordx4 v[72:75], v112, s[60:61] sc1\n"
    "v_mfma_f32_32x32x16_bf16 a[32:47], v[76:79], v[84:87], a[32:47]\n"
    "v_mfma_f32_32x32x16_bf16 a[48:63], v[76:79], v[88:91], a[48:63]\n"
    "global_load_dwordx4 v[76:79], v113, s[60:61] sc1\n"
    "v_mfma_f32_32x32x16_bf16 a[64:79], v[80:83], v[84:87], a[64:79]\n"
    "global_load_dwordx4 v[80:83], v114, s[60:61] sc1\n"
    "s_add_u32 s60, s60, 20480\n"
    "s_addc_u32 s61, s61, 0\n"
    "global_load_dwordx4 v[84:87], v115, s[58:59] offset:2048 sc1\n"
    "global_load_dwordx4 v[88:91], v116, s[58:59] offset:2048 sc1\n"
    "s_add_u32 s58, s58, 4096\n"
    "s_addc_u32 s59, s59, 0\n"
    "s_waitcnt vmcnt(5)\n"
    "v_mfma_f32_32x32x16_bf16 a[0:15], v[52:55], v[64:67], a[0:15]\n"
    "v_mfma_f32_32x32x16_bf16 a[16:31], v[52:55], v[68:71], a[16:31]\n"
    "global_load_dwordx4 v[52:55], v112, s[60:61] sc1\n"
    "v_mfma_f32_32x32x16_bf16 a[32:47], v[56:59], v[64:67], a[32:47]\n"
    "v_mfma_f32_32x32x16_bf16 a[48:63], v[56:59], v[68:71], a[48:63]\n"
    "global_load_dwordx4 v[56:59], v113, s[60:61] sc1\n"
    "v_mfma_f32_32x32x16_bf16 a[64:79], v[60:63], v[64:67], a[64:79]\n"
    "global_load_dwordx4 v[60:63], v114, s[60:61] sc1\n"
    "s_add_u32 s60, s60, 20480\n"
    "s_addc_u32 s61, s61, 0\n"
    "global_load_dwordx4 v[64:67], v115, s[58:59] offset:0 sc1\n"
    "global_load_dwordx4 v[68:71], v116, s[58:59] offset:0 sc1\n"
    "s_waitcnt vmcnt(5)\n"
    "v_mfma_f32_32x32x16_bf16 a[0:15], v[72:75], v[84:87], a[0:15]\n"
    "v_mfma_f32_32x32x16_bf16 a[16:31], v[72:75], v[88:91], a[16:31]\n"
    "global_load_dwordx4 v[72:75], v112, s[60:61] sc1\n"
    "v_mfma_f32_32x32x16_bf16 a[32:47], v[76:79], v[84:87], a[32:47]\n"
    "v_mfma_f32_32x32x16_bf16 a[48:63], v[76:79], v[88:91], a[48:63]\n"
    "global_load_dwordx4 v[76:79], v113, s[60:61] sc1\n"
    "v_mfma_f32_32x32x16_bf16 a[64:79], v[80:83], v[84:87], a[64:79]\n"
    "global_load_dwordx4 v[80:83], v114, s[60:61] sc1\n"
    "s_add_u32 s60, s60, 20480\n"
    "s_addc_u32 s61, s61, 0\n"
    "global_load_dwordx4 v[84:87], v115, s[58:59] offset:2048 sc1\n"
    "global_load_dwordx4 v[88:91], v116, s[58:59] offset:2048 sc1\n"
    "s_add_u32 s58, s58, 4096\n"
    "s_addc_u32 s59, s59, 0\n"
    "s_waitcnt vmcnt(5)\n"
    "v_mfma_f32_32x32x16_bf16 a[0:15], v[52:55], v[64:67], a[0:15]\n"
    "v_mfma_f32_32x32x16_bf16 a[16:31], v[52:55], v[68:71], a[16:31]\n"
    "global_load_dwordx4 v[52:55], v112, s[60:61] sc1\n"
    "v_mfma_f32_32x32x16_bf16 a[32:47], v[56:59], v[64:67], a[32:47]\n"
    "v_mfma_f32_32x32x16_bf16 a[48:63], v[56:59], v[68:71], a[48:63]\n"
    "global_load_dwordx4 v[56:59], v113, s[60:61] sc1\n"
    "v_mfma_f32_32x32x16_bf16 a[64:79], v[60:63], v[64:67], a[64:79]\n"
    "global_load_dwordx4 v[60:63], v114, s[60:61] sc1\n"
    "s_add_u32 s60, s60, 20480\n"
    "s_addc_u32 s61, s61, 0\n"
    "global_load_dwordx4 v[64:67], v115, s[58:59] offset:0 sc1\n"
    "global_load_dwordx4 v[68:71], v116, s[58:59] offset:0 sc1\n"
    "s_waitcnt vmcnt(5)\n"
    "v_mfma_f32_32x32x16_bf16 a[0:15], v[72:75], v[84:87], a[0:15]\n"
    "v_mfma_f32_32x32x16_bf16 a[16:31], v[72:75], v[88:91], a[16:31]\n"
    "global_load_dwordx4 v[72:75], v112, s[60:61] sc1\n"
    "v_mfma_f32_32x32x16_bf16 a[32:47], v[76:79], v[84:87], a[32:47]\n"
    "v_mfma_f32_32x32x16_bf16 a[48:63], v[76:79], v[88:91], a[48:63]\n"
    "global_load_dwordx4 v[76:79], v113, s[60:61] sc1\n"
    "v_mfma_f32_32x32x16_bf16 a[64:79], v[80:83], v[84:87], a[64:79]\n"
    "global_load_dwordx4 v[80:83], v114, s[60:61] sc1\n"
    "s_add_u32 s60, s60, 20480\n"
    "s_addc_u32 s61, s61, 0\n"
    "global_load_dwordx4 v[84:87], v115, s[58:59] offset:2048 sc1\n"
    "global_load_dwordx4 v[88:91], v116, s[58:59] offset:2048 sc1\n"
    "s_add_u32 s58, s58, 4096\n"
    "s_addc_u32 s59, s59, 0\n"
    "s_waitcnt vmcnt(5)\n"
    "v_mfma_f32_32x32x16_bf16 a[0:15], v[52:55], v[64:67], a[0:15]\n"
    "v_mfma_f32_32x32x16_bf16 a[16:31], v[52:55], v[68:71], a[16:31]\n"
    "global_load_dwordx4 v[52:55], v112, s[60:61] sc1\n"
    "v_mfma_f32_32x32x16_bf16 a[32:47], v[56:59], v[64:67], a[32:47]\n"
    "v_mfma_f32_32x32x16_bf16 a[48:63], v[56:59], v[68:71], a[48:63]\n"
    "global_load_dwordx4 v[56:59], v113, s[60:61] sc1\n"
    "v_mfma_f32_32x32x16_bf16 a[64:79], v[60:63], v[64:67], a[64:79]\n"
    "global_load_dwordx4 v[60:63], v114, s[60:61] sc1\n"
    "s_add_u32 s60, s60, 20480\n"
    "s_addc_u32 s61, s61, 0\n"
    "global_load_dwordx4 v[64:67], v115, s[58:59] offset:0 sc1\n"
    "global_load_dwordx4 v[68:71], v116, s[58:59] offset:0 sc1\n"
    "s_waitcnt vmcnt(5)\n"
    "v_mfma_f32_32x32x16_bf16 a[0:15], v[72:75], v[84:87], a[0:15]\n"
    "v_mfma_f32_32x32x16_bf16 a[16:31], v[72:75], v[88:91], a[16:31]\n"
    "global_load_dwordx4 v[72:75], v112, s[60:61] sc1\n"
    "v_mfma_f32_32x32x16_bf16 a[32:47], v[76:79], v[84:87], a[32:47]\n"
    "v_mfma_f32_32x32x16_bf16 a[48:63], v[76:79], v[88:91], a[48:63]\n"
    "global_load_dwordx4 v[76:79], v113, s[60:61] sc1\n"
    "v_mfma_f32_32x32x16_bf16 a[64:79], v[80:83], v[84:87], a[64:79]\n"
    "global_load_dwordx4 v[80:83], v114, s[60:61] sc1\n"
    "s_add_u32 s60, s60, 20480\n"
    "s_addc_u32 s61, s61, 0\n"
    "global_load_dwordx4 v[84:87], v115, s[58:59] offset:2048 sc1\n"
    "global_load_dwordx4 v[88:91], v116, s[58:59] offset:2048 sc1\n"
    "s_add_u32 s58, s58, 4096\n"
    "s_addc_u32 s59, s59, 0\n"
    "s_waitcnt vmcnt(5)\n"
    "v_mfma_f32_32x32x16_bf16 a[0:15], v[52:55], v[64:67], a[0:15]\n"
    "v_mfma_f32_32x32x16_bf16 a[16:31], v[52:55], v[68:71], a[16:31]\n"
    "global_load_dwordx4 v[52:55], v112, s[60:61] sc1\n"
    "v_mfma_f32_32x32x16_bf16 a[32:47], v[56:59], v[64:67], a[32:47]\n"
    "v_mfma_f32_32x32x16_bf16 a[48:63], v[56:59], v[68:71], a[48:63]\n"
    "global_load_dwordx4 v[56:59], v113, s[60:61] sc1\n"
    "v_mfma_f32_32x32x16_bf16 a[64:79], v[60:63], v[64:67], a[64:79]\n"
    "global_load_dwordx4 v[60:63], v114, s[60:61] sc1\n"
    "s_add_u32 s60, s60, 20480\n"
    "s_addc_u32 s61, s61, 0\n"
    "global_load_dwordx4 v[64:67], v115, s[58:59] offset:0 sc1\n"
    "global_load_dwordx4 v[68:71], v116, s[58:59] offset:0 sc1\n"
    "s_waitcnt vmcnt(5)\n"
    "v_mfma_f32_32x32x16_bf16 a[0:15], v[72:75], v[84:87], a[0:15]\n"
    "v_mfma_f32_32x32x16_bf16 a[16:31], v[72:75], v[88:91], a[16:31]\n"
    "global_load_dwordx4 v[72:75], v112, s[60:61] sc1\n"
    "v_mfma_f32_32x32x16_bf16 a[32:47], v[76:79], v[84:87], a[32:47]\n"
    "v_mfma_f32_32x32x16_bf16 a[48:63], v[76:79], v[88:91], a[48:63]\n"
    "global_load_dwordx4 v[76:79], v113, s[60:61] sc1\n"
    "v_mfma_f32_32x32x16_bf16 a[64:79], v[80:83], v[84:87], a[64:79]\n"
    "global_load_dwordx4 v[80:83], v114, s[60:61] sc1\n"
    "s_add_u32 s60, s60, 20480\n"
    "s_addc_u32 s61, s61, 0\n"
    "global_load_dwordx4 v[84:87], v115, s[58:59] offset:2048 sc1\n"
    "global_load_dwordx4 v[88:91], v116, s[58:59] offset:2048 sc1\n"
    "s_add_u32 s58, s58, 4096\n"
    "s_addc_u32 s59, s59, 0\n"
    "s_waitcnt vmcnt(5)\n"
    "v_mfma_f32_32x32x16_bf16 a[0:15], v[52:55], v[64:67], a[0:15]\n"
    "v_mfma_f32_32x32x16_bf16 a[16:31], v[52:55], v[68:71], a[16:31]\n"
    "global_load_dwordx4 v[52:55], v112, s[60:61] sc1\n"
    "v_mfma_f32_32x32x16_bf16 a[32:47], v[56:59], v[64:67], a[32:47]\n"
    "v_mfma_f32_32x32x16_bf16 a[48:63], v[56:59], v[68:71], a[48:63]\n"
    "global_load_dwordx4 v[56:59], v113, s[60:61] sc1\n"
    "v_mfma_f32_32x32x16_bf16 a[64:79], v[60:63], v[64:67], a[64:79]\n"
    "global_load_dwordx4 v[60:63], v114, s[60:61] sc1\n"
    "s_add_u32 s60, s60, 20480\n"
    "s_addc_u32 s61, s61, 0\n"
    "global_load_dwordx4 v[64:67], v115, s[58:59] offset:0 sc1\n"
    "global_load_dwordx4 v[68:71], v116, s[58:59] offset:0 sc1\n"
    "s_waitcnt vmcnt(5)\n"
    "v_mfma_f32_32x32x16_bf16 a[0:15], v[72:75], v[84:87], a[0:15]\n"
    "v_mfma_f32_32x32x16_bf16 a[16:31], v[72:75], v[88:91], a[16:31]\n"
    "global_load_dwordx4 v[72:75], v112, s[60:61] sc1\n"
    "v_mfma_f32_32x32x16_bf16 a[32:47], v[76:79], v[84:87], a[32:47]\n"
    "v_mfma_f32_32x32x16_bf16 a[48:63], v[76:79], v[88:91], a[48:63]\n"
    "global_load_dwordx4 v[76:79], v113, s[60:61] sc1\n"
    "v_mfma_f32_32x32x16_bf16 a[64:79], v[80:83], v[84:87], a[64:79]\n"
    "global_load_dwordx4 v[80:83], v114, s[60:61] sc1\n"
    "s_add_u32 s60, s60, 20480\n"
    "s_addc_u32 s61, s61, 0\n"
    "global_load_dwordx4 v[84:87], v115, s[58:59] offset:2048 sc1\n"
    "global_load_dwordx4 v[88:91], v116, s[58:59] offset:2048 sc1\n"
    "s_add_u32 s58, s58, 4096\n"
    "s_addc_u32 s59, s59, 0\n"
    "s_waitcnt vmcnt(5)\n"
    "v_mfma_f32_32x32x16_bf16 a[0:15], v[52:55], v[64:67], a[0:15]\n"
    "v_mfma_f32_32x32x16_bf16 a[16:31], v[52:55], v[68:71], a[16:31]\n"
    "global_load_dwordx4 v[52:55], v112, s[60:61] sc1\n"
    "v_mfma_f32_32x32x16_bf16 a[32:47], v[56:59], v[64:67], a[32:47]\n"
    "v_mfma_f32_32x32x16_bf16 a[48:63], v[56:59], v[68:71], a[48:63]\n"
    "global_load_dwordx4 v[56:59], v113, s[60:61] sc1\n"
    "v_mfma_f32_32x32x16_bf16 a[64:79], v[60:63], v[64:67], a[64:79]\n"
    "global_load_dwordx4 v[60:63], v114, s[60:61] sc1\n"
    "s_add_u32 s60, s60, 20480\n"
    "s_addc_u32 s61, s61, 0\n"
    "global_load_dwordx4 v[64:67], v115, s[58:59] offset:0 sc1\n"
    "global_load_dwordx4 v[68:71], v116, s[58:59] offset:0 sc1\n"
    "s_waitcnt vmcnt(5)\n"
    "v_mfma_f32_32x32x16_bf16 a[0:15], v[72:75], v[84:87], a[0:15]\n"
    "v_mfma_f32_32x32x16_bf16 a[16:31], v[72:75], v[88:91], a[16:31]\n"
    "global_load_dwordx4 v[72:75], v112, s[60:61] sc1\n"
    "v_mfma_f32_32x32x16_bf16 a[32:47], v[76:79], v[84:87], a[32:47]\n"
    "v_mfma_f32_32x32x16_bf16 a[48:63], v[76:79], v[88:91], a[48:63]\n"
    "global_load_dwordx4 v[76:79], v113, s[60:61] sc1\n"
    "v_mfma_f32_32x32x16_bf16 a[64:79], v[80:83], v[84:87], a[64:79]\n"
    "global_load_dwordx4 v[80:83], v114, s[60:61] sc1\n"
    "s_add_u32 s60, s60, 20480\n"
    "s_addc_u32 s61, s61, 0\n"
    "global_load_dwordx4 v[84:87], v115, s[58:59] offset:2048 sc1\n"
    "global_load_dwordx4 v[88:91], v116, s[58:59] offset:2048 sc1\n"
    "s_add_u32 s58, s58, 4096\n"
    "s_addc_u32 s59, s59, 0\n"
    "s_waitcnt vmcnt(5)\n"
    "v_mfma_f32_32x32x16_bf16 a[0:15], v[52:55], v[64:67], a[0:15]\n"
    "v_mfma_f32_32x32x16_bf16 a[16:31], v[52:55], v[68:71], a[16:31]\n"
    "global_load_dwordx4 v[52:55], v112, s[60:61] sc1\n"
    "v_mfma_f32_32x32x16_bf16 a[32:47], v[56:59], v[64:67], a[32:47]\n"
    "v_mfma_f32_32x32x16_bf16 a[48:63], v[56:59], v[68:71], a[48:63]\n"
    "global_load_dwordx4 v[56:59], v113, s[60:61] sc1\n"
    "v_mfma_f32_32x32x16_bf16 a[64:79], v[60:63], v[64:67], a[64:79]\n"
    "global_load_dwordx4 v[60:63], v114, s[60:61] sc1\n"
    "s_add_u32 s60, s60, 20480\n"
    "s_addc_u32 s61, s61, 0\n"
    "global_load_dwordx4 v[64:67], v115, s[58:59] offset:0 sc1\n"
    "global_load_dwordx4 v[68:71], v116, s[58:59] offset:0 sc1\n"
    "s_waitcnt vmcnt(5)\n"
    "v_mfma_f32_32x32x16_bf16 a[0:15], v[72:75], v[84:87], a[0:15]\n"
    "v_mfma_f32_32x32x16_bf16 a[16:31], v[72:75], v[88:91], a[16:31]\n"
    "global_load_dwordx4 v[72:75], v112, s[60:61] sc1\n"
    "v_mfma_f32_32x32x16_bf16 a[32:47], v[76:79], v[84:87], a[32:47]\n"
    "v_mfma_f32_32x32x16_bf16 a[48:63], v[76:79], v[88:91], a[48:63]\n"
    "global_load_dwordx4 v[76:79], v113, s[60:61] sc1\n"
    "v_mfma_f32_32x32x16_bf16 a[64:79], v[80:83], v[84:87], a[64:79]\n"
    "global_load_dwordx4 v[80:83], v114, s[60:61] sc1\n"
    "s_add_u32 s60, s60, 20480\n"
    "s_addc_u32 s61, s61, 0\n"
    "global_load_dwordx4 v[84:87], v115, s[58:59] offset:2048 sc1\n"
    "global_load_dwordx4 v[88:91], v116, s[58:59] offset:2048 sc1\n"
    "s_add_u32 s58, s58, 4096\n"
    "s_addc_u32 s59, s59, 0\n"
    "s_waitcnt vmcnt(5)\n"
    "v_mfma_f32_32x32x16_bf16 a[0:15], v[52:55], v[64:67], a[0:15]\n"
    "v_mfma_f32_32x32x16_bf16 a[16:31], v[52:55], v[68:71], a[16:31]\n"
    "global_load_dwordx4 v[52:55], v112, s[60:61] sc1\n"
    "v_mfma_f32_32x32x16_bf16 a[32:47], v[56:59], v[64:67], a[32:47]\n"
    "v_mfma_f32_32x32x16_bf16 a[48:63], v[56:59], v[68:71], a[48:63]\n"
    "global_load_dwordx4 v[56:59], v113, s[60:61] sc1\n"
    "v_mfma_f32_32x32x16_bf16 a[64:79], v[60:63], v[64:67], a[64:79]\n"
    "global_load_dwordx4 v[60:63], v114, s[60:61] sc1\n"
    "s_add_u32 s60, s60, 20480\n"
    "s_addc_u32 s61, s61, 0\n"
    "global_load_dwordx4 v[64:67], v115, s[58:59] offset:0 sc1\n"
    "global_load_dwordx4 v[68:71], v116, s[58:59] offset:0 sc1\n"
    "s_waitcnt vmcnt(5)\n"
    "v_mfma_f32_32x32x16_bf16 a[0:15], v[72:75], v[84:87], a[0:15]\n"
    "v_mfma_f32_32x32x16_bf16 a[16:31], v[72:75], v[88:91], a[16:31]\n"
    "global_load_dwordx4 v[72:75], v112, s[60:61] sc1\n"
    "v_mfma_f32_32x32x16_bf16 a[32:47], v[76:79], v[84:87], a[32:47]\n"
    "v_mfma_f32_32x32x16_bf16 a[48:63], v[76:79], v[88:91], a[48:63]\n"
    "global_load_dwordx4 v[76:79], v113, s[60:61] sc1\n"
    "v_mfma_f32_32x32x16_bf16 a[64:79], v[80:83], v[84:87], a[64:79]\n"
    "global_load_dwordx4 v[80:83], v114, s[60:61] sc1\n"
    "s_add_u32 s60, s60, 20480\n"
    "s_addc_u32 s61, s61, 0\n"
    "global_load_dwordx4 v[84:87], v115, s[58:59] offset:2048 sc1\n"
    "global_load_dwordx4 v[88:91], v116, s[58:59] offset:2048 sc1\n"
    "s_add_u32 s58, s58, 4096\n"
    "s_addc_u32 s59, s59, 0\n"
    "s_waitcnt vmcnt(5)\n"
    "v_mfma_f32_32x32x16_bf16 a[0:15], v[52:55], v[64:67], a[0:15]\n"
    "v_mfma_f32_32x32x16_bf16 a[16:31], v[52:55], v[68:71], a[16:31]\n"
    "global_load_dwordx4 v[52:55], v112, s[60:61] sc1\n"
    "v_mfma_f32_32x32x16_bf16 a[32:47], v[56:59], v[64:67], a[32:47]\n"
    "v_mfma_f32_32x32x16_bf16 a[48:63], v[56:59], v[68:71], a[48:63]\n"
    "global_load_dwordx4 v[56:59], v113, s[60:61] sc1\n"
    "v_mfma_f32_32x32x16_bf16 a[64:79], v[60:63], v[64:67], a[64:79]\n"
    "global_load_dwordx4 v[60:63], v114, s[60:61] sc1\n"
    "s_add_u32 s60, s60, 20480\n"
    "s_addc_u32 s61, s61, 0\n"
    "global_load_dwordx4 v[64:67], v115, s[58:59] offset:0 sc1\n"
    "global_load_dwordx4 v[68:71], v116, s[58:59] offset:0 sc1\n"
    "s_waitcnt vmcnt(5)\n"
    "v_mfma_f32_32x32x16_bf16 a[0:15], v[72:75], v[84:87], a[0:15]\n"
    "v_mfma_f32_32x32x16_bf16 a[16:31], v[72:75], v[88:91], a[16:31]\n"
    "v_mfma_f32_32x32x16_bf16 a[32:47], v[76:79], v[84:87], a[32:47]\n"
    "v_mfma_f32_32x32x16_bf16 a[48:63], v[76:79], v[88:91], a[48:63]\n"
    "v_mfma_f32_32x32x16_bf16 a[64:79], v[80:83], v[84:87], a[64:79]\n"
    "s_waitcnt vmcnt(0)\n"
    "v_mfma_f32_32x32x16_bf16 a[0:15], v[52:55], v[64:67], a[0:15]\n"
    "v_mfma_f32_32x32x16_bf16 a[16:31], v[52:55], v[68:71], a[16:31]\n"
    "v_mfma_f32_32x32x16_bf16 a[32:47], v[56:59], v[64:67], a[32:47]\n"
    "v_mfma_f32_32x32x16_bf16 a[48:63], v[56:59], v[68:71], a[48:63]\n"
    "v_mfma_f32_32x32x16_bf16 a[64:79], v[60:63], v[64:67], a[64:79]\n"
    "s_mov_b64 s[60:61], s[56:57]\n"
    "global_load_dwordx4 v[48:51], v112, s[60:61] sc1\n"
    "global_load_dwordx4 v[52:55], v113, s[60:61] sc1\n"
    "global_load_dwordx4 v[56:59], v114, s[60:61] sc1\n"
    "s_add_u32 s60, s60, 20480\n"
    "s_addc_u32 s61, s61, 0\n"
    "global_load_dwordx4 v[60:63], v112, s[60:61] sc1\n"
    "global_load_dwordx4 v[64:67], v113, s[60:61] sc1\n"
    "global_load_dwordx4 v[68:71], v114, s[60:61] sc1\n"
    "s_add_u32 s60, s60, 20480\n"
    "s_addc_u32 s61, s61, 0\n"
    "s_setprio 3\n"
    "s_mov_b64 s[58:59], %[obA]\n"
    "s_mov_b64 s[54:55], %[obB]\n"
    "s_add_u32 s56, s58, 8192\n"
    "s_addc_u32 s57, s59, 0\n"
    "s_add_u32 s52, s54, 8192\n"
    "s_addc_u32 s53, s55, 0\n"
    "s_mov_b32 s62, 0x100000\n"
    "WG%=:\n"
    "ds_read_b32 v123, v121\n"
    "s_waitcnt lgkmcnt(0)\n"
    "v_readfirstlane_b32 s63, v123\n"
    "s_sub_u32 s62, s62, 1\n"
    "s_cmp_eq_u32 s62, 0\n"
    "s_cbranch_scc1 WH%=\n"
    "s_cmp_ge_u32 s63, 4\n"
    "s_cbranch_scc1 WH%=\n"
    "s_sleep 3\n"
    "s_branch WG%=\n"
    "WH%=:\n"
    "s_cmp_lg_u32 %[own], 0\n"
    "s_cbranch_scc1 WL%=\n"
    "s_mov_b32 s62, 0x10000\n"
    "WPA%=:\n"
    "global_load_dwordx2 v[72:73], v122, %[ofb] sc1\n"
    "s_waitcnt vmcnt(0)\n"
    "v_cmp_eq_u64 vcc, %[ep], v[72:73]\n"
    "s_nop 3\n"
    "s_cmp_eq_u64 vcc, exec\n"
    "s_cbranch_scc1 WQA%=\n"
    "s_sub_u32 s62, s62, 1\n"
    "s_cmp_lg_u32 s62, 0\n"
    "s_cbranch_scc1 WPA%=\n"
    "WQA%=:\n"
    "global_load_dwordx4 v[72:75], v115, s[58:59] offset:-4096 sc1\n"
    "global_load_dwordx4 v[76:79], v116, s[58:59] offset:-4096 sc1\n"
    "global_load_dwordx4 v[80:83], v115, s[58:59] offset:-2048 sc1\n"
    "global_load_dwordx4 v[84:87], v116, s[58:59] offset:-2048 sc1\n"
    "global_load_dwordx4 v[88:91], v115, s[58:59] offset:0 sc1\n"
    "global_load_dwordx4 v[92:95], v116, s[58:59] offset:0 sc1\n"
    "global_load_dwordx4 v[96:99], v115, s[58:59] offset:2048 sc1\n"
    "global_load_dwordx4 v[100:103], v116, s[58:59] offset:2048 sc1\n"
    "global_load_dwordx4 v[104:107], v115, s[56:57] offset:-4096 sc1\n"
    "global_load_dwordx4 v[108:111], v116, s[56:57] offset:-4096 sc1\n"
    "s_waitcnt vmcnt(8)\n"
    "v_max3_u32 v123, v72, v73, v74\n"
    "v_max3_u32 v124, v75, v76, v77\n"
    "v_max3_u32 v123, v123, v78, v79\n"
    "v_max_u32 v123, v123, v124\n"
    "v_cmp_eq_u32 vcc, -1, v123\n"
    "s_nop 0\n"
    "s_cbranch_vccnz WX0%=\n"
    "WV0%=:\n"
    "v_mfma_f32_32x32x16_bf16 a[0:15], a[80:83], v[72:75], a[0:15]\n"
    "v_mfma_f32_32x32x16_bf16 a[16:31], a[80:83], v[76:79], a[16:31]\n"
    "v_mfma_f32_32x32x16_bf16 a[32:47], a[84:87], v[72:75], a[32:47]\n"
    "s_waitcnt vmcnt(6)\n"
    "v_max3_u32 v123, v80, v81, v82\n"
    "v_max3_u32 v124, v83, v84, v85\n"
    "v_max3_u32 v123, v123, v86, v87\n"
    "v_max_u32 v123, v123, v124\n"
    "v_cmp_eq_u32 vcc, -1, v123\n"
    "s_nop 0\n"
    "s_cbranch_vccnz WX1%=\n"
    "WV1%=:\n"
    "v_mfma_f32_32x32x16_bf16 a[48:63], a[84:87], v[76:79], a[48:63]\n"
    "v_mfma_f32_32x32x16_bf16 a[64:79], a[88:91], v[72:75], a[64:79]\n"
    "global_load_dwordx4 v[72:75], v115, s[54:55] offset:-4096 sc1\n"
    "global_load_dwordx4 v[76:79], v116, s[54:55] offset:-4096 sc1\n"
    "v_mfma_f32_32x32x16_bf16 a[0:15], a[92:95], v[80:83], a[0:15]\n"
    "v_mfma_f32_32x32x16_bf16 a[16:31], a[92:95], v[84:87], a[16:31]\n"
    "v_mfma_f32_32x32x16_bf16 a[32:47], a[96:99], v[80:83], a[32:47]\n"
    "s_waitcnt vmcnt(6)\n"
    "v_max3_u32 v123, v88, v89, v90\n"
    "v_max3_u32 v124, v91, v92, v93\n"
    "v_max3_u32 v123, v123, v94, v95\n"
    "v_max_u32 v123, v123, v124\n"
    "v_cmp_eq_u32 vcc, -1, v123\n"
    "s_nop 0\n"
    "s_cbranch_vccnz WX2%=\n"
    "WV2%=:\n"
    "v_mfma_f32_32x32x16_bf16 a[48:63], a[96:99], v[84:87], a[48:63]\n"
    "v_mfma_f32_32x32x16_bf16 a[64:79], a[100:103], v[80:83], a[64:79]\n"
    "global_load_dwordx4 v[80:83], v115, s[54:55] offset:-2048 sc1\n"
    "global_load_dwordx4 v[84:87], v116, s[54:55] offset:-2048 sc1\n"
    "v_mfma_f32_32x32x16_bf16 a[0:15], a[104:107], v[88:91], a[0:15]\n"
    "v_mfma_f32_32x32x16_bf16 a[16:31], a[104:107], v[92:95], a[16:31]\n"
    "v_mfma_f32_32x32x16_bf16 a[32:47], a[108:111], v[88:91], a[32:47]\n"
    "s_waitcnt vmcnt(6)\n"
    "v_max3_u32 v123, v96, v97, v98\n"
    "v_max3_u32 v124, v99, v100, v101\n"
    "v_max3_u32 v123, v123, v102, v103\n"
    "v_max_u32 v123, v123, v124\n"
    "v_cmp_eq_u32 vcc, -1, v123\n"
    "s_nop 0\n"
    "s_cbranch_vccnz WX3%=\n"
    "WV3%=:\n"
    "v_mfma_f32_32x32x16_bf16 a[48:63], a[108:111], v[92:95], a[48:63]\n"
    "v_mfma_f32_32x32x16_bf16 a[64:79], a[112:115], v[88:91], a[64:79]\n"
    "global_load_dwordx4 v[88:91], v115, s[54:55] offset:0 sc1\n"
    "global_load_dwordx4 v[92:95], v116, s[54:55] offset:0 sc1\n"
    "v_mfma_f32_32x32x16_bf16 a[0:15], a[116:119], v[96:99], a[0:15]\n"
    "v_mfma_f32_32x32x16_bf16 a[16:31], a[116:119], v[100:103], a[16:31]\n"
    "v_mfma_f32_32x32x16_bf16 a[32:47], a[120:123], v[96:99], a[32:47]\n"
    "s_waitcnt vmcnt(6)\n"
    "v_max3_u32 v123, v104, v105, v106\n"
    "v_max3_u32 v124, v107, v108, v109\n"
    "v_max3_u32 v123, v123, v110, v111\n"
    "v_max_u32 v123, v123, v124\n"
    "v_cmp_eq_u32 vcc, -1, v123\n"
    "s_nop 0\n"
    "s_cbranch_vccnz WX4%=\n"
    "WV4%=:\n"
    "v_mfma_f32_32x32x16_bf16 a[48:63], a[120:123], v[100:103], a[48:63]\n"
    "v_mfma_f32_32x32x16_bf16 a[64:79], a[124:127], v[96:99], a[64:79]\n"
    "global_load_dwordx4 v[96:99], v115, s[54:55] offset:2048 sc1\n"
    "global_load_dwordx4 v[100:103], v116, s[54:55] offset:2048 sc1\n"
    "v_mfma_f32_32x32x16_bf16 a[0:15], v[0:3], v[104:107], a[0:15]\n"
    "v_mfma_f32_32x32x16_bf16 a[16:31], v[0:3], v[108:111], a[16:31]\n"
    "v_mfma_f32_32x32x16_bf16 a[32:47], v[4:7], v[104:107], a[32:47]\n"
    "v_mfma_f32_32x32x16_bf16 a[48:63], v[4:7], v[108:111], a[48:63]\n"
    "v_mfma_f32_32x32x16_bf16 a[64:79], v[8:11], v[104:107], a[64:79]\n"
    "global_load_dwordx4 v[104:107], v115, s[52:53] offset:-4096 sc1\n"
    "global_load_dwordx4 v[108:111], v116, s[52:53] offset:-4096 sc1\n"
    "s_branch WM%=\n"
    "WL%=:\n"
    "global_load_dwordx2 v[112:113], v122, %[ofb] sc1\n"
    "ds_read_b128 v[72:75], v115 offset:20480\n"
    "ds_read_b128 v[76:79], v116 offset:20480\n"
    "ds_read_b128 v[80:83], v115 offset:22528\n"
    "ds_read_b128 v[84:87], v116 offset:22528\n"
    "ds_read_b128 v[88:91], v115 offset:24576\n"
    "ds_read_b128 v[92:95], v116 offset:24576\n"
    "ds_read_b128 v[96:99], v115 offset:26624\n"
    "ds_read_b128 v[100:103], v116 offset:26624\n"
    "ds_read_b128 v[104:107], v115 offset:28672\n"
    "ds_read_b128 v[108:111], v116 offset:28672\n"
    "s_waitcnt vmcnt(1) lgkmcnt(8)\n"
    "v_mfma_f32_32x32x16_bf16 a[0:15], a[80:83], v[72:75], a[0:15]\n"
    "v_mfma_f32_32x32x16_bf16 a[16:31], a[80:83], v[76:79], a[16:31]\n"
    "v_mfma_f32_32x32x16_bf16 a[32:47], a[84:87], v[72:75], a[32:47]\n"
    "v_mfma_f32_32x32x16_bf16 a[48:63], a[84:87], v[76:79], a[48:63]\n"
    "v_mfma_f32_32x32x16_bf16 a[64:79], a[88:91], v[72:75], a[64:79]\n"
    "s_waitcnt lgkmcnt(0)\n"
    "v_mfma_f32_32x32x16_bf16 a[0:15], a[92:95], v[80:83], a[0:15]\n"
    "v_mfma_f32_32x32x16_bf16 a[16:31], a[92:95], v[84:87], a[16:31]\n"
    "v_mfma_f32_32x32x16_bf16 a[32:47], a[96:99], v[80:83], a[32:47]\n"
    "v_mfma_f32_32x32x16_bf16 a[48:63], a[96:99], v[84:87], a[48:63]\n"
    "v_mfma_f32_32x32x16_bf16 a[64:79], a[100:103], v[80:83], a[64:79]\n"
    "v_mfma_f32_32x32x16_bf16 a[0:15], a[104:107], v[88:91], a[0:15]\n"
    "v_mfma_f32_32x32x16_bf16 a[16:31], a[104:107], v[92:95], a[16:31]\n"
    "v_mfma_f32_32x32x16_bf16 a[32:47], a[108:111], v[88:91], a[32:47]\n"
    "v_mfma_f32_32x32x16_bf16 a[48:63], a[108:111], v[92:95], a[48:63]\n"
    "v_mfma_f32_32x32x16_bf16 a[64:79], a[112:115], v[88:91], a[64:79]\n"
    "s_waitcnt vmcnt(0)\n"
    "v_cmp_eq_u64 vcc, %[ep], v[112:113]\n"
    "s_nop 3\n"
    "s_cmp_eq_u64 vcc, exec\n"
    "s_cbranch_scc1 WK%=\n"
    "v_mfma_f32_32x32x16_bf16 a[0:15], a[116:119], v[96:99], a[0:15]\n"
    "v_mfma_f32_32x32x16_bf16 a[16:31], a[116:119], v[100:103], a[16:31]\n"
    "v_mfma_f32_32x32x16_bf16 a[32:47], a[120:123], v[96:99], a[32:47]\n"
    "v_mfma_f32_32x32x16_bf16 a[48:63], a[120:123], v[100:103], a[48:63]\n"
    "v_mfma_f32_32x32x16_bf16 a[64:79], a[124:127], v[96:99], a[64:79]\n"
    "v_mfma_f32_32x32x16_bf16 a[0:15], v[0:3], v[104:107], a[0:15]\n"
    "v_mfma_f32_32x32x16_bf16 a[16:31], v[0:3], v[108:111], a[16:31]\n"
    "v_mfma_f32_32x32x16_bf16 a[32:47], v[4:7], v[104:107], a[32:47]\n"
    "v_mfma_f32_32x32x16_bf16 a[48:63], v[4:7], v[108:111], a[48:63]\n"
    "v_mfma_f32_32x32x16_bf16 a[64:79], v[8:11], v[104:107], a[64:79]\n"
    "s_mov_b32 s62, 0x10000\n"
    "WPB%=:\n"
    "global_load_dwordx2 v[72:73], v122, %[ofb] sc1\n"
    "s_waitcnt vmcnt(0)\n"
    "v_cmp_eq_u64 vcc, %[ep], v[72:73]\n"
    "s_nop 3\n"
    "s_cmp_eq_u64 vcc, exec\n"
    "s_cbranch_scc1 WQB%=\n"
    "s_sub_u32 s62, s62, 1\n"
    "s_cmp_lg_u32 s62, 0\n"
    "s_cbranch_scc1 WPB%=\n"
    "WQB%=:\n"
    "global_load_dwordx4 v[72:75], v115, s[54:55] offset:-4096 sc1\n"
    "global_load_dwordx4 v[76:79], v116, s[54:55] offset:-4096 sc1\n"
    "global_load_dwordx4 v[80:83], v115, s[54:55] offset:-2048 sc1\n"
    "global_load_dwordx4 v[84:87], v116, s[54:55] offset:-2048 sc1\n"
    "global_load_dwordx4 v[88:91], v115, s[54:55] offset:0 sc1\n"
    "global_load_dwordx4 v[92:95], v116, s[54:55] offset:0 sc1\n"
    "global_load_dwordx4 v[96:99], v115, s[54:55] offset:2048 sc1\n"
    "global_load_dwordx4 v[100:103], v116, s[54:55] offset:2048 sc1\n"
    "global_load_dwordx4 v[104:107], v115, s[52:53] offset:-4096 sc1\n"
    "global_load_dwordx4 v[108:111], v116, s[52:53] offset:-4096 sc1\n"
    "s_branch WM%=\n"
    "WK%=:\n"
    "global_load_dwordx4 v[72:75], v115, s[54:55] offset:-4096 sc1\n"
    "global_load_dwordx4 v[76:79], v116, s[54:55] offset:-4096 sc1\n"
    "global_load_dwordx4 v[80:83], v115, s[54:55] offset:-2048 sc1\n"
    "global_load_dwordx4 v[84:87], v116, s[54:55] offset:-2048 sc1\n"
    "global_load_dwordx4 v[88:91], v115, s[54:55] offset:0 sc1\n"
    "global_load_dwordx4 v[92:95], v116, s[54:55] offset:0 sc1\n"
    "v_mfma_f32_32x32x16_bf16 a[0:15], a[116:119], v[96:99], a[0:15]\n"
    "v_mfma_f32_32x32x16_bf16 a[16:31], a[116:119], v[100:103], a[16:31]\n"
    "v_mfma_f32_32x32x16_bf16 a[32:47], a[120:123], v[96:99], a[32:47]\n"
    "v_mfma_f32_32x32x16_bf16 a[48:63], a[120:123], v[100:103], a[48:63]\n"
    "v_mfma_f32_32x32x16_bf16 a[64:79], a[124:127], v[96:99], a[64:79]\n"
    "global_load_dwordx4 v[96:99], v115, s[54:55] offset:2048 sc1\n"
    "global_load_dwordx4 v[100:103], v116, s[54:55] offset:2048 sc1\n"
    "v_mfma_f32_32x32x16_bf16 a[0:15], v[0:3], v[104:107], a[0:15]\n"
    "v_mfma_f32_32x32x16_bf16 a[16:31], v[0:3], v[108:111], a[16:31]\n"
    "v_mfma_f32_32x32x16_bf16 a[32:47], v[4:7], v[104:107], a[32:47]\n"
    "v_mfma_f32_32x32x16_bf16 a[48:63], v[4:7], v[108:111], a[48:63]\n"
    "v_mfma_f32_32x32x16_bf16 a[64:79], v[8:11], v[104:107], a[64:79]\n"
    "global_load_dwordx4 v[104:107], v115, s[52:53] offset:-4096 sc1\n"
    "global_load_dwordx4 v[108:111], v116, s[52:53] offset:-4096 sc1\n"
    "WM%=:\n"
    "s_waitcnt vmcnt(8)\n"
    "v_max3_u32 v123, v72, v73, v74\n"
    "v_max3_u32 v124, v75, v76, v77\n"
    "v_max3_u32 v123, v123, v78, v79\n"
    "v_max_u32 v123, v123, v124\n"
    "v_cmp_eq_u32 vcc, -1, v123\n"
    "s_nop 0\n"
    "s_cbranch_vccnz WX5%=\n"
    "WV5%=:\n"
    "v_mfma_f32_32x32x16_bf16 a[0:15], v[12:15], v[72:75], a[0:15]\n"
    "v_mfma_f32_32x32x16_bf16 a[16:31], v[12:15], v[76:79], a[16:31]\n"
    "v_mfma_f32_32x32x16_bf16 a[32:47], v[16:19], v[72:75], a[32:47]\n"
    "s_waitcnt vmcnt(6)\n"
    "v_max3_u32 v123, v80, v81, v82\n"
    "v_max3_u32 v124, v83, v84, v85\n"
    "v_max3_u32 v123, v123, v86, v87\n"
    "v_max_u32 v123, v123, v124\n"
    "v_cmp_eq_u32 vcc, -1, v123\n"
    "s_nop 0\n"
    "s_cbranch_vccnz WX6%=\n"
    "WV6%=:\n"
    "v_mfma_f32_32x32x16_bf16 a[48:63], v[16:19], v[76:79], a[48:63]\n"
    "v_mfma_f32_32x32x16_bf16 a[64:79], v[20:23], v[72:75], a[64:79]\n"
    "v_mfma_f32_32x32x16_bf16 a[0:15], v[24:27], v[80:83], a[0:15]\n"
    "v_mfma_f32_32x32x16_bf16 a[16:31], v[24:27], v[84:87], a[16:31]\n"
    "s_waitcnt vmcnt(4)\n"
    "v_max3_u32 v123, v88, v89, v90\n"
    "v_max3_u32 v124, v91, v92, v93\n"
    "v_max3_u32 v123, v123, v94, v95\n"
    "v_max_u32 v123, v123, v124\n"
    "v_cmp_eq_u32 vcc, -1, v123\n"
    "s_nop 0\n"
    "s_cbranch_vccnz WX7%=\n"
    "WV7%=:\n"
    "v_mfma_f32_32x32x16_bf16 a[32:47], v[28:31], v[80:83], a[32:47]\n"
    "v_mfma_f32_32x32x16_bf16 a[0:15], v[36:39], v[88:91], a[0:15]\n"
    "v_mfma_f32_32x32x16_bf16 a[16:31], v[36:39], v[92:95], a[16:31]\n"
    "s_waitcnt vmcnt(2)\n"
    "v_max3_u32 v123, v96, v97, v98\n"
    "v_max3_u32 v124, v99, v100, v101\n"
    "v_max3_u32 v123, v123, v102, v103\n"
    "v_max_u32 v123, v123, v124\n"
    "v_cmp_eq_u32 vcc, -1, v123\n"
    "s_nop 0\n"
    "s_cbranch_vccnz WX8%=\n"
    "WV8%=:\n"
    "v_mfma_f32_32x32x16_bf16 a[0:15], v[48:51], v[96:99], a[0:15]\n"
    "v_mfma_f32_32x32x16_bf16 a[16:31], v[48:51], v[100:103], a[16:31]\n"
    "s_waitcnt vmcnt(0)\n"
    "v_max3_u32 v123, v104, v105, v106\n"
    "v_max3_u32 v124, v107, v108, v109\n"
    "v_max3_u32 v123, v123, v110, v111\n"
    "v_max_u32 v123, v123, v124\n"
    "v_cmp_eq_u32 vcc, -1, v123\n"
    "s_nop 0\n"
    "s_cbranch_vccnz WX9%=\n"
    "WV9%=:\n"
    "v_mfma_f32_32x32x16_bf16 a[0:15], v[60:63], v[104:107], a[0:15]\n"
    "v_mfma_f32_32x32x16_bf16 a[16:31], v[60:63], v[108:111], a[16:31]\n"
    "v_mfma_f32_32x32x16_bf16 a[32:47], v[40:43], v[88:91], a[32:47]\n"
    "v_mfma_f32_32x32x16_bf16 a[32:47], v[52:55], v[96:99], a[32:47]\n"
    "v_mfma_f32_32x32x16_bf16 a[32:47], v[64:67], v[104:107], a[32:47]\n"
    "v_mfma_f32_32x32x16_bf16 a[48:63], v[28:31], v[84:87], a[48:63]\n"
    "s_nop 6\n"
    "ds_write_b128 v117, a[0:3] offset:0\n"
    "ds_write_b128 v118, a[4:7] offset:0\n"
    "v_mfma_f32_32x32x16_bf16 a[48:63], v[40:43], v[92:95], a[48:63]\n"
    "ds_write_b128 v119, a[8:11] offset:0\n"
    "ds_write_b128 v120, a[12:15] offset:0\n"
    "v_mfma_f32_32x32x16_bf16 a[48:63], v[52:55], v[100:103], a[48:63]\n"
    "ds_write_b128 v117, a[16:19] offset:4096\n"
    "ds_write_b128 v118, a[20:23] offset:4096\n"
    "v_mfma_f32_32x32x16_bf16 a[48:63], v[64:67], v[108:111], a[48:63]\n"
    "ds_write_b128 v119, a[24:27] offset:4096\n"
    "ds_write_b128 v120, a[28:31] offset:4096\n"
    "v_mfma_f32_32x32x16_bf16 a[64:79], v[32:35], v[80:83], a[64:79]\n"
    "ds_write_b128 v117, a[32:35] offset:8192\n"
    "ds_write_b128 v118, a[36:39] offset:8192\n"
    "v_mfma_f32_32x32x16_bf16 a[64:79], v[44:47], v[88:91], a[64:79]\n"
    "ds_write_b128 v119, a[40:43] offset:8192\n"
    "ds_write_b128 v120, a[44:47] offset:8192\n"
    "v_mfma_f32_32x32x16_bf16 a[64:79], v[56:59], v[96:99], a[64:79]\n"
    "v_mfma_f32_32x32x16_bf16 a[64:79], v[68:71], v[104:107], a[64:79]\n"
    "v_mov_b32 v123, 1\n"
    "s_nop 3\n"
    "ds_write_b128 v117, a[48:51] offset:12288\n"
    "ds_write_b128 v118, a[52:55] offset:12288\n"
    "ds_write_b128 v119, a[56:59] offset:12288\n"
    "ds_write_b128 v120, a[60:63] offset:12288\n"
    "s_nop 3\n"
    "ds_write_b128 v117, a[64:67] offset:16384\n"
    "ds_write_b128 v118, a[68:71] offset:16384\n"
    "ds_write_b128 v119, a[72:75] offset:16384\n"
    "ds_write_b128 v120, a[76:79] offset:16384\n"
    "s_nop 3\n"
    "s_waitcnt lgkmcnt(0)\n"
    "s_branch WE%=\n"
    "WX0%=:\n"
    "s_mov_b32 s62, 0x4000\n"
    "WR0%=:\n"
    "global_load_dwordx4 v[72:75], v115, s[58:59] offset:-4096 sc1\n"
    "global_load_dwordx4 v[76:79], v116, s[58:59] offset:-4096 sc1\n"
    "s_waitcnt vmcnt(0)\n"
    "v_max3_u32 v123, v72, v73, v74\n"
    "v_max3_u32 v124, v75, v76, v77\n"
    "v_max3_u32 v123, v123, v78, v79\n"
    "v_max_u32 v123, v123, v124\n"
    "v_cmp_eq_u32 vcc, -1, v123\n"
    "s_nop 0\n"
    "s_sub_u32 s62, s62, 1\n"
    "s_cmp_eq_u32 s62, 0\n"
    "s_cbranch_scc1 WV0%=\n"
    "s_cbranch_vccnz WR0%=\n"
    "s_branch WV0%=\n"
    "WX1%=:\n"
    "s_mov_b32 s62, 0x4000\n"
    "WR1%=:\n"
    "global_load_dwordx4 v[80:83], v115, s[58:59] offset:-2048 sc1\n"
    "global_load_dwordx4 v[84:87], v116, s[58:59] offset:-2048 sc1\n"
    "s_waitcnt vmcnt(0)\n"
    "v_max3_u32 v123, v80, v81, v82\n"
    "v_max3_u32 v124, v83, v84, v85\n"
    "v_max3_u32 v123, v123, v86, v87\n"
    "v_max_u32 v123, v123, v124\n"
    "v_cmp_eq_u32 vcc, -1, v123\n"
    "s_nop 0\n"
    "s_sub_u32 s62, s62, 1\n"
    "s_cmp_eq_u32 s62, 0\n"
    "s_cbranch_scc1 WV1%=\n"
    "s_cbranch_vccnz WR1%=\n"
    "s_branch WV1%=\n"
    "WX2%=:\n"
    "s_mov_b32 s62, 0x4000\n"
    "WR2%=:\n"
    "global_load_dwordx4 v[88:91], v115, s[58:59] offset:0 sc1\n"
    "global_load_dwordx4 v[92:95], v116, s[58:59] offset:0 sc1\n"
    "s_waitcnt vmcnt(0)\n"
    "v_max3_u32 v123, v88, v89, v90\n"
    "v_max3_u32 v124, v91, v92, v93\n"
    "v_max3_u32 v123, v123, v94, v95\n"
    "v_max_u32 v123, v123, v124\n"
    "v_cmp_eq_u32 vcc, -1, v123\n"
    "s_nop 0\n"
    "s_sub_u32 s62, s62, 1\n"
    "s_cmp_eq_u32 s62, 0\n"
    "s_cbranch_scc1 WV2%=\n"
    "s_cbranch_vccnz WR2%=\n"
    "s_branch WV2%=\n"
    "WX3%=:\n"
    "s_mov_b32 s62, 0x4000\n"
    "WR3%=:\n"
    "global_load_dwordx4 v[96:99], v115, s[58:59] offset:2048 sc1\n"
    "global_load_dwordx4 v[100:103], v116, s[58:59] offset:2048 sc1\n"
    "s_waitcnt vmcnt(0)\n"
    "v_max3_u32 v123, v96, v97, v98\n"
    "v_max3_u32 v124, v99, v100, v101\n"
    "v_max3_u32 v123, v123, v102, v103\n"
    "v_max_u32 v123, v123, v124\n"
    "v_cmp_eq_u32 vcc, -1, v123\n"
    "s_nop 0\n"
    "s_sub_u32 s62, s62, 1\n"
    "s_cmp_eq_u32 s62, 0\n"
    "s_cbranch_scc1 WV3%=\n"
    "s_cbranch_vccnz WR3%=\n"
    "s_branch WV3%=\n"
    "WX4%=:\n"
    "s_mov_b32 s62, 0x4000\n"
    "WR4%=:\n"
    "global_load_dwordx4 v[104:107], v115, s[56:57] offset:-4096 sc1\n"
    "global_load_dwordx4 v[108:111], v116, s[56:57] offset:-4096 sc1\n"
    "s_waitcnt vmcnt(0)\n"
    "v_max3_u32 v123, v104, v105, v106\n"
    "v_max3_u32 v124, v107, v108, v109\n"
    "v_max3_u32 v123, v123, v110, v111\n"
    "v_max_u32 v123, v123, v124\n"
    "v_cmp_eq_u32 vcc, -1, v123\n"
    "s_nop 0\n"
    "s_sub_u32 s62, s62, 1\n"
    "s_cmp_eq_u32 s62, 0\n"
    "s_cbranch_scc1 WV4%=\n"
    "s_cbranch_vccnz WR4%=\n"
    "s_branch WV4%=\n"
    "WX5%=:\n"
    "s_mov_b32 s62, 0x4000\n"
    "WR5%=:\n"
    "global_load_dwordx4 v[72:75], v115, s[54:55] offset:-4096 sc1\n"
    "global_load_dwordx4 v[76:79], v116, s[54:55] offset:-4096 sc1\n"
    "s_waitcnt vmcnt(0)\n"
    "v_max3_u32 v123, v72, v73, v74\n"
    "v_max3_u32 v124, v75, v76, v77\n"
    "v_max3_u32 v123, v123, v78, v79\n"
    "v_max_u32 v123, v123, v124\n"
    "v_cmp_eq_u32 vcc, -1, v123\n"
    "s_nop 0\n"
    "s_sub_u32 s62, s62, 1\n"
    "s_cmp_eq_u32 s62, 0\n"
    "s_cbranch_scc1 WV5%=\n"
    "s_cbranch_vccnz WR5%=\n"
    "s_branch WV5%=\n"
    "WX6%=:\n"
    "s_mov_b32 s62, 0x4000\n"
    "WR6%=:\n"
    "global_load_dwordx4 v[80:83], v115, s[54:55] offset:-2048 sc1\n"
    "global_load_dwordx4 v[84:87], v116, s[54:55] offset:-2048 sc1\n"
    "s_waitcnt vmcnt(0)\n"
    "v_max3_u32 v123, v80, v81, v82\n"
    "v_max3_u32 v124, v83, v84, v85\n"
    "v_max3_u32 v123, v123, v86, v87\n"
    "v_max_u32 v123, v123, v124\n"
    "v_cmp_eq_u32 vcc, -1, v123\n"
    "s_nop 0\n"
    "s_sub_u32 s62, s62, 1\n"
    "s_cmp_eq_u32 s62, 0\n"
    "s_cbranch_scc1 WV6%=\n"
    "s_cbranch_vccnz WR6%=\n"
    "s_branch WV6%=\n"
    "WX7%=:\n"
    "s_mov_b32 s62, 0x4000\n"
    "WR7%=:\n"
    "global_load_dwordx4 v[88:91], v115, s[54:55] offset:0 sc1\n"
    "global_load_dwordx4 v[92:95], v116, s[54:55] offset:0 sc1\n"
    "s_waitcnt vmcnt(0)\n"
    "v_max3_u32 v123, v88, v89, v90\n"
    "v_max3_u32 v124, v91, v92, v93\n"
    "v_max3_u32 v123, v123, v94, v95\n"
    "v_max_u32 v123, v123, v124\n"
    "v_cmp_eq_u32 vcc, -1, v123\n"
    "s_nop 0\n"
    "s_sub_u32 s62, s62, 1\n"
    "s_cmp_eq_u32 s62, 0\n"
    "s_cbranch_scc1 WV7%=\n"
    "s_cbranch_vccnz WR7%=\n"
    "s_branch WV7%=\n"
    "WX8%=:\n"
    "s_mov_b32 s62, 0x4000\n"
    "WR8%=:\n"
    "global_load_dwordx4 v[96:99], v115, s[54:55] offset:2048 sc1\n"
    "global_load_dwordx4 v[100:103], v116, s[54:55] offset:2048 sc1\n"
    "s_waitcnt vmcnt(0)\n"
    "v_max3_u32 v123, v96, v97, v98\n"
    "v_max3_u32 v124, v99, v100, v101\n"
    "v_max3_u32 v123, v123, v102, v103\n"
    "v_max_u32 v123, v123, v124\n"
    "v_cmp_eq_u32 vcc, -1, v123\n"
    "s_nop 0\n"
    "s_sub_u32 s62, s62, 1\n"
    "s_cmp_eq_u32 s62, 0\n"
    "s_cbranch_scc1 WV8%=\n"
    "s_cbranch_vccnz WR8%=\n"
    "s_branch WV8%=\n"
    "WX9%=:\n"
    "s_mov_b32 s62, 0x4000\n"
    "WR9%=:\n"
    "global_load_dwordx4 v[104:107], v115, s[52:53] offset:-4096 sc1\n"
    "global_load_dwordx4 v[108:111], v116, s[52:53] offset:-4096 sc1\n"
    "s_waitcnt vmcnt(0)\n"
    "v_max3_u32 v123, v104, v105, v106\n"
    "v_max3_u32 v124, v107, v108, v109\n"
    "v_max3_u32 v123, v123, v110, v111\n"
    "v_max_u32 v123, v123, v124\n"
    "v_cmp_eq_u32 vcc, -1, v123\n"
    "s_nop 0\n"
    "s_sub_u32 s62, s62, 1\n"
    "s_cmp_eq_u32 s62, 0\n"
    "s_cbranch_scc1 WV9%=\n"
    "s_cbranch_vccnz WR9%=\n"
    "s_branch WV9%=\n"
    "WE%=:\n"
      : "+{v[120:123]}"(g120), "+{v[112:119]}"(g112) : [hfb] "s"(ws + W_HF + (b & 1) * 8192 + g * 256),
        [ab] "s"(ws + W_H + (size_t)g * 221184 + q * 27 * 2048), [wb] "s"(ws + W_WF + q * 27 * 20480), [wob] "s"(ws + W_WF + (108 + 10 * q + 5 * sw) * 20480), [wd] "s"(sw ? -204800 : 0), [own] "s"(own),
        [obA] "s"(obA), [obB] "s"(obB), [ofb] "s"(ws + W_OF + (b & 1) * 8192 + g * 256), [ep] "s"(epoch)
      : "v0", "v1", "v2", "v3", "v4", "v5", "v6", "v7", "v8", "v9", "v10", "v11", "v12", "v13", "v14", "v15", "v16", "v17", "v18", "v19", "v20", "v21", "v22", "v23", "v24", "v25", "v26", "v27", "v28", "v29", "v30", "v31", "v32", "v33", "v34", "v35", "v36", "v37", "v38", "v39", "v40", "v41", "v42", "v43", "v44", "v45", "v46", "v47", "v48", "v49", "v50", "v51", "v52", "v53", "v54", "v55", "v56", "v57", "v58", "v59", "v60", "v61", "v62", "v63", "v64", "v65", "v66", "v67", "v68", "v69", "v70", "v71", "v72", "v73", "v74", "v75", "v76", "v77", "v78", "v79", "v80", "v81", "v82", "v83", "v84", "v85", "v86", "v87", "v88", "v89", "v90", "v91", "v92", "v93", "v94", "v95", "v96", "v97", "v98", "v99", "v100", "v101", "v102", "v103", "v104", "v105", "v106", "v107", "v108", "v109", "v110", "v111", "v124", "a0", "a1", "a2", "a3", "a4", "a5", "a6", "a7", "a8", "a9", "a10", "a11", "a12", "a13", "a14", "a15", "a16", "a17", "a18", "a19", "a20", "a21", "a22", "a23", "a24", "a25", "a26", "a27", "a28", "a29", "a30", "a31", "a32", "a33", "a34", "a35", "a36", "a37", "a38", "a39", "a40", "a41", "a42", "a43", "a44", "a45", "a46", "a47", "a48", "a49", "a50", "a51", "a52", "a53", "a54", "a55", "a56", "a57", "a58", "a59", "a60", "a61", "a62", "a63", "a64", "a65", "a66", "a67", "a68", "a69", "a70", "a71", "a72", "a73", "a74", "a75", "a76", "a77", "a78", "a79", "a80", "a81", "a82", "a83", "a84", "a85", "a86", "a87", "a88", "a89", "a90", "a91", "a92", "a93", "a94", "a95", "a96", "a97", "a98", "a99", "a100", "a101", "a102", "a103", "a104", "a105", "a106", "a107", "a108", "a109", "a110", "a111", "a112", "a113", "a114", "a115", "a116", "a117", "a118", "a119", "a120", "a121", "a122", "a123", "a124", "a125", "a126", "a127", "s49", "s50", "s51", "s52", "s53", "s54", "s55", "s56", "s57", "s58", "s59", "s60", "s61", "s62", "s63", "s64", "vcc", "scc", "memory");
    asm volatile("s_waitcnt lgkmcnt(0)\n\ts_barrier" ::: "memory");
    __builtin_amdgcn_endpgm();
    __builtin_unreachable();
  }
  {
    u32x4 a84, a110; u32x8 a92, xbv; u32x2 a122; u32 junk0; u64 epoch;
    asm volatile("" : "={v[84:87]}"(a84), "={v[92:99]}"(a92), "={v[110:113]}"(a110), "={v[114:121]}"(xbv), "={v[122:123]}"(a122), "={v54}"(junk0));
    asm volatile("" : "={s[66:67]}"(epoch));
    u32x4 q0a, q0b; u32x2 q1; u32 bad; u32x8 xres0; u32x2 xres1;
    asm volatile(
    "s_mov_b32 s64, m0\n"
    "ds_read_b32 v90, v92\n"
    "ds_read_b32 v91, v92 offset:128\n"
    "v_mov_b32 v88, 1\n"
    "v_mov_b32 v89, %[flb]\n"
    "s_add_u32 s65, %[soff], 245760\n"
    "s_waitcnt vmcnt(11)\n"
    "ds_read_b64_tr_b16 v[32:33], v122 offset:0\n"
    "ds_read_b64_tr_b16 v[34:35], v122 offset:256\n"
    "ds_read_b128 v[40:43], v114 offset:0\n"
    "ds_read_b128 v[44:47], v118 offset:0\n"
    "s_waitcnt vmcnt(10)\n"
    "ds_read_b64_tr_b16 v[36:37], v122 offset:1024\n"
    "ds_read_b64_tr_b16 v[38:39], v122 offset:1280\n"
    "ds_read_b128 v[48:51], v115 offset:1024\n"
    "ds_read_b128 v[52:55], v119 offset:1024\n"
    "s_waitcnt lgkmcnt(4)\n"
    "v_mfma_f32_32x32x16_bf16 v[0:15], v[32:35], v[40:43], 0\n"
    "s_add_u32 m0, %[m0b], 0\n"
    "s_nop 0\n"
    "buffer_load_dwordx4 v113, %[srd], s65 offen lds\n"
    "s_add_u32 s65, s65, 20480\n"
    "v_mfma_f32_32x32x16_bf16 v[16:31], v[32:35], v[44:47], 0\n"
    "s_waitcnt vmcnt(10)\n"
    "ds_read_b64_tr_b16 v[32:33], v122 offset:2048\n"
    "ds_read_b64_tr_b16 v[34:35], v122 offset:2304\n"
    "ds_read_b128 v[40:43], v116 offset:2048\n"
    "ds_read_b128 v[44:47], v120 offset:2048\n"
    "s_waitcnt lgkmcnt(4)\n"
    "v_mfma_f32_32x32x16_bf16 v[0:15], v[36:39], v[48:51], v[0:15]\n"
    "s_add_u32 m0, %[m0b], 1024\n"
    "s_nop 0\n"
    "buffer_load_dwordx4 v113, %[srd], s65 offen lds\n"
    "s_add_u32 s65, s65, 20480\n"
    "v_mfma_f32_32x32x16_bf16 v[16:31], v[36:39], v[52:55], v[16:31]\n"
    "s_waitcnt vmcnt(10)\n"
    "ds_read_b64_tr_b16 v[36:37], v122 offset:3072\n"
    "ds_read_b64_tr_b16 v[38:39], v122 offset:3328\n"
    "ds_read_b128 v[48:51], v117 offset:3072\n"
    "ds_read_b128 v[52:55], v121 offset:3072\n"
    "s_waitcnt lgkmcnt(4)\n"
    "v_mfma_f32_32x32x16_bf16 v[0:15], v[32:35], v[40:43], v[0:15]\n"
    "s_add_u32 m0, %[m0b], 2048\n"
    "s_nop 0\n"
    "buffer_load_dwordx4 v113, %[srd], s65 offen lds\n"
    "s_add_u32 s65, s65, 20480\n"
    "v_mfma_f32_32x32x16_bf16 v[16:31], v[32:35], v[44:47], v[16:31]\n"
    "s_waitcnt vmcnt(10)\n"
    "ds_read_b64_tr_b16 v[32:33], v122 offset:4096\n"
    "ds_read_b64_tr_b16 v[34:35], v122 offset:4352\n"
    "ds_read_b128 v[40:43], v114 offset:4096\n"
    "ds_read_b128 v[44:47], v118 offset:4096\n"
    "s_waitcnt lgkmcnt(4)\n"
    "v_mfma_f32_32x32x16_bf16 v[0:15], v[36:39], v[48:51], v[0:15]\n"
    "s_add_u32 m0, %[m0b], 3072\n"
    "s_nop 0\n"
    "buffer_load_dwordx4 v113, %[srd], s65 offen lds\n"
    "s_add_u32 s65, s65, 20480\n"
    "v_mfma_f32_32x32x16_bf16 v[16:31], v[36:39], v[52:55], v[16:31]\n"
    "s_waitcnt vmcnt(10)\n"
    "ds_read_b64_tr_b16 v[36:37], v122 offset:5120\n"
    "ds_read_b64_tr_b16 v[38:39], v122 offset:5376\n"
    "ds_read_b128 v[48:51], v115 offset:5120\n"
    "ds_read_b128 v[52:55], v119 offset:5120\n"
    "s_waitcnt lgkmcnt(4)\n"
    "v_mfma_f32_32x32x16_bf16 v[0:15], v[32:35], v[40:43], v[0:15]\n"
    "s_add_u32 m0, %[m0b], 4096\n"
    "s_nop 0\n"
    "buffer_load_dwordx4 v113, %[srd], s65 offen lds\n"
    "s_add_u32 s65, s65, 20480\n"
    "v_mfma_f32_32x32x16_bf16 v[16:31], v[32:35], v[44:47], v[16:31]\n"
    "s_waitcnt vmcnt(10)\n"
    "ds_read_b64_tr_b16 v[32:33], v123 offset:0\n"
    "ds_read_b64_tr_b16 v[34:35], v123 offset:256\n"
    "ds_read_b128 v[40:43], v116 offset:6144\n"
    "ds_read_b128 v[44:47], v120 offset:6144\n"
    "s_waitcnt lgkmcnt(4)\n"
    "v_mfma_f32_32x32x16_bf16 v[0:15], v[36:39], v[48:51], v[0:15]\n"
    "s_add_u32 m0, %[m0b], 5120\n"
    "s_nop 0\n"
    "buffer_load_dwordx4 v113, %[srd], s65 offen lds\n"
    "s_add_u32 s65, s65, 20480\n"
    "v_mfma_f32_32x32x16_bf16 v[16:31], v[36:39], v[52:55], v[16:31]\n"
    "s_waitcnt vmcnt(10)\n"
    "ds_read_b64_tr_b16 v[36:37], v123 offset:1024\n"
    "ds_read_b64_tr_b16 v[38:39], v123 offset:1280\n"
    "ds_read_b128 v[48:51], v117 offset:7168\n"
    "ds_read_b128 v[52:55], v121 offset:7168\n"
    "s_waitcnt lgkmcnt(4)\n"
    "v_mfma_f32_32x32x16_bf16 v[0:15], v[32:35], v[40:43], v[0:15]\n"
    "s_add_u32 m0, %[m0c], 0\n"
    "s_nop 0\n"
    "buffer_load_dwordx4 v113, %[srd], s65 offen lds\n"
    "s_add_u32 s65, s65, 20480\n"
    "v_mfma_f32_32x32x16_bf16 v[16:31], v[32:35], v[44:47], v[16:31]\n"
    "s_waitcnt vmcnt(10)\n"
    "ds_read_b64_tr_b16 v[32:33], v123 offset:2048\n"
    "ds_read_b64_tr_b16 v[34:35], v123 offset:2304\n"
    "ds_read_b128 v[40:43], v114 offset:8192\n"
    "ds_read_b128 v[44:47], v118 offset:8192\n"
    "s_waitcnt lgkmcnt(4)\n"
    "v_mfma_f32_32x32x16_bf16 v[0:15], v[36:39], v[48:51], v[0:15]\n"
    "s_add_u32 m0, %[m0c], 1024\n"
    "s_nop 0\n"
    "buffer_load_dwordx4 v113, %[srd], s65 offen lds\n"
    "s_add_u32 s65, s65, 20480\n"
    "v_mfma_f32_32x32x16_bf16 v[16:31], v[36:39], v[52:55], v[16:31]\n"
    "s_waitcnt vmcnt(10)\n"
    "ds_read_b64_tr_b16 v[36:37], v123 offset:3072\n"
    "ds_read_b64_tr_b16 v[38:39], v123 offset:3328\n"
    "ds_read_b128 v[48:51], v115 offset:9216\n"
    "ds_read_b128 v[52:55], v119 offset:9216\n"
    "s_waitcnt lgkmcnt(4)\n"
    "v_mfma_f32_32x32x16_bf16 v[0:15], v[32:35], v[40:43], v[0:15]\n"
    "s_add_u32 m0, %[m0c], 2048\n"
    "s_nop 0\n"
    "buffer_load_dwordx4 v113, %[srd], s65 offen lds\n"
    "s_add_u32 s65, s65, 20480\n"
    "v_mfma_f32_32x32x16_bf16 v[16:31], v[32:35], v[44:47], v[16:31]\n"
    "s_waitcnt vmcnt(10)\n"
    "ds_read_b64_tr_b16 v[32:33], v123 offset:4096\n"
    "ds_read_b64_tr_b16 v[34:35], v123 offset:4352\n"
    "ds_read_b128 v[40:43], v116 offset:10240\n"
    "ds_read_b128 v[44:47], v120 offset:10240\n"
    "s_waitcnt lgkmcnt(4)\n"
    "v_mfma_f32_32x32x16_bf16 v[0:15], v[36:39], v[48:51], v[0:15]\n"
    "s_add_u32 m0, %[m0c], 3072\n"
    "s_nop 0\n"
    "buffer_load_dwordx4 v113, %[srd], s65 offen lds\n"
    "s_add_u32 s65, s65, 20480\n"
    "v_mfma_f32_32x32x16_bf16 v[16:31], v[36:39], v[52:55], v[16:31]\n"
    "s_waitcnt vmcnt(10)\n"
    "ds_read_b64_tr_b16 v[36:37], v123 offset:5120\n"
    "ds_read_b64_tr_b16 v[38:39], v123 offset:5376\n"
    "ds_read_b128 v[48:51], v117 offset:11264\n"
    "ds_read_b128 v[52:55], v121 offset:11264\n"
    "s_waitcnt lgkmcnt(4)\n"
    "v_mfma_f32_32x32x16_bf16 v[0:15], v[32:35], v[40:43], v[0:15]\n"
    "s_add_u32 m0, %[m0c], 4096\n"
    "s_nop 0\n"
    "buffer_load_dwordx4 v113, %[srd], s65 offen lds\n"
    "s_add_u32 s65, s65, 20480\n"
    "v_mfma_f32_32x32x16_bf16 v[16:31], v[32:35], v[44:47], v[16:31]\n"
    "s_waitcnt vmcnt(10)\n"
    "ds_read_b64_tr_b16 v[32:33], v122 offset:0\n"
    "ds_read_b64_tr_b16 v[34:35], v122 offset:256\n"
    "ds_read_b128 v[40:43], v114 offset:12288\n"
    "ds_read_b128 v[44:47], v118 offset:12288\n"
    "s_waitcnt lgkmcnt(4)\n"
    "v_mfma_f32_32x32x16_bf16 v[0:15], v[36:39], v[48:51], v[0:15]\n"
    "s_add_u32 m0, %[m0c], 5120\n"
    "s_nop 0\n"
    "buffer_load_dwordx4 v113, %[srd], s65 offen lds\n"
    "s_add_u32 s65, s65, 20480\n"
    "v_mfma_f32_32x32x16_bf16 v[16:31], v[36:39], v[52:55], v[16:31]\n"
    "s_waitcnt vmcnt(10)\n"
    "ds_read_b64_tr_b16 v[36:37], v122 offset:1024\n"
    "ds_read_b64_tr_b16 v[38:39], v122 offset:1280\n"
    "ds_read_b128 v[48:51], v115 offset:13312\n"
    "ds_read_b128 v[52:55], v119 offset:13312\n"
    "s_waitcnt lgkmcnt(4)\n"
    "v_mfma_f32_32x32x16_bf16 v[0:15], v[32:35], v[40:43], v[0:15]\n"
    "s_add_u32 m0, %[m0b], 0\n"
    "s_nop 0\n"
    "buffer_load_dwordx4 v113, %[srd], s65 offen lds\n"
    "s_add_u32 s65, s65, 20480\n"
    "v_mfma_f32_32x32x16_bf16 v[16:31], v[32:35], v[44:47], v[16:31]\n"
    "s_waitcnt vmcnt(10)\n"
    "ds_read_b64_tr_b16 v[32:33], v122 offset:2048\n"
    "ds_read_b64_tr_b16 v[34:35], v122 offset:2304\n"
    "ds_read_b128 v[40:43], v116 offset:14336\n"
    "ds_read_b128 v[44:47], v120 offset:14336\n"
    "s_waitcnt lgkmcnt(4)\n"
    "v_mfma_f32_32x32x16_bf16 v[0:15], v[36:39], v[48:51], v[0:15]\n"
    "s_add_u32 m0, %[m0b], 1024\n"
    "s_nop 0\n"
    "buffer_load_dwordx4 v113, %[srd], s65 offen lds\n"
    "s_add_u32 s65, s65, 20480\n"
    "v_mfma_f32_32x32x16_bf16 v[16:31], v[36:39], v[52:55], v[16:31]\n"
    "s_waitcnt vmcnt(10)\n"
    "ds_read_b64_tr_b16 v[36:37], v122 offset:3072\n"
    "ds_read_b64_tr_b16 v[38:39], v122 offset:3328\n"
    "ds_read_b128 v[48:51], v117 offset:15360\n"
    "ds_read_b128 v[52:55], v121 offset:15360\n"
    "s_waitcnt lgkmcnt(4)\n"
    "v_mfma_f32_32x32x16_bf16 v[0:15], v[32:35], v[40:43], v[0:15]\n"
    "s_add_u32 m0, %[m0b], 2048\n"
    "s_nop 0\n"
    "buffer_load_dwordx4 v113, %[srd], s65 offen lds\n"
    "s_add_u32 s65, s65, 20480\n"
    "v_mfma_f32_32x32x16_bf16 v[16:31], v[32:35], v[44:47], v[16:31]\n"
    "s_waitcnt vmcnt(10)\n"
    "ds_read_b64_tr_b16 v[32:33], v122 offset:4096\n"
    "ds_read_b64_tr_b16 v[34:35], v122 offset:4352\n"
    "ds_read_b128 v[40:43], v114 offset:16384\n"
    "ds_read_b128 v[44:47], v118 offset:16384\n"
    "s_waitcnt lgkmcnt(4)\n"
    "v_mfma_f32_32x32x16_bf16 v[0:15], v[36:39], v[48:51], v[0:15]\n"
    "s_add_u32 m0, %[m0b], 3072\n"
    "s_nop 0\n"
    "buffer_load_dwordx4 v113, %[srd], s65 offen lds\n"
    "s_add_u32 s65, s65, 20480\n"
    "v_mfma_f32_32x32x16_bf16 v[16:31], v[36:39], v[52:55], v[16:31]\n"
    "s_waitcnt vmcnt(10)\n"
    "ds_read_b64_tr_b16 v[36:37], v122 offset:5120\n"
    "ds_read_b64_tr_b16 v[38:39], v122 offset:5376\n"
    "ds_read_b128 v[48:51], v115 offset:17408\n"
    "ds_read_b128 v[52:55], v119 offset:17408\n"
    "s_waitcnt lgkmcnt(4)\n"
    "v_mfma_f32_32x32x16_bf16 v[0:15], v[32:35], v[40:43], v[0:15]\n"
    "s_add_u32 m0, %[m0b], 4096\n"
    "s_nop 0\n"
    "buffer_load_dwordx4 v113, %[srd], s65 offen lds\n"
    "s_add_u32 s65, s65, 20480\n"
    "v_mfma_f32_32x32x16_bf16 v[16:31], v[32:35], v[44:47], v[16:31]\n"
    "s_waitcnt vmcnt(10)\n"
    "ds_read_b64_tr_b16 v[32:33], v123 offset:0\n"
    "ds_read_b64_tr_b16 v[34:35], v123 offset:256\n"
    "ds_read_b128 v[40:43], v116 offset:18432\n"
    "ds_read_b128 v[44:47], v120 offset:18432\n"
    "s_waitcnt lgkmcnt(4)\n"
    "v_mfma_f32_32x32x16_bf16 v[0:15], v[36:39], v[48:51], v[0:15]\n"
    "s_add_u32 m0, %[m0b], 5120\n"
    "s_nop 0\n"
    "buffer_load_dwordx4 v113, %[srd], s65 offen lds\n"
    "s_add_u32 s65, s65, 20480\n"
    "v_mfma_f32_32x32x16_bf16 v[16:31], v[36:39], v[52:55], v[16:31]\n"
    "s_waitcnt vmcnt(10)\n"
    "ds_read_b64_tr_b16 v[36:37], v123 offset:1024\n"
    "ds_read_b64_tr_b16 v[38:39], v123 offset:1280\n"
    "ds_read_b128 v[48:51], v117 offset:19456\n"
    "ds_read_b128 v[52:55], v121 offset:19456\n"
    "s_waitcnt lgkmcnt(4)\n"
    "v_mfma_f32_32x32x16_bf16 v[0:15], v[32:35], v[40:43], v[0:15]\n"
    "s_add_u32 m0, %[m0c], 0\n"
    "s_nop 0\n"
    "buffer_load_dwordx4 v113, %[srd], s65 offen lds\n"
    "s_add_u32 s65, s65, 20480\n"
    "v_mfma_f32_32x32x16_bf16 v[16:31], v[32:35], v[44:47], v[16:31]\n"
    "s_waitcnt vmcnt(10)\n"
    "ds_read_b64_tr_b16 v[32:33], v123 offset:2048\n"
    "ds_read_b64_tr_b16 v[34:35], v123 offset:2304\n"
    "ds_read_b128 v[40:43], v114 offset:34752\n"
    "ds_read_b128 v[44:47], v118 offset:34752\n"
    "s_waitcnt lgkmcnt(4)\n"
    "v_mfma_f32_32x32x16_bf16 v[0:15], v[36:39], v[48:51], v[0:15]\n"
    "s_add_u32 m0, %[m0c], 1024\n"
    "s_nop 0\n"
    "buffer_load_dwordx4 v113, %[srd], s65 offen lds\n"
    "s_add_u32 s65, s65, 20480\n"
    "v_mfma_f32_32x32x16_bf16 v[16:31], v[36:39], v[52:55], v[16:31]\n"
    "s_waitcnt vmcnt(10)\n"
    "ds_read_b64_tr_b16 v[36:37], v123 offset:3072\n"
    "ds_read_b64_tr_b16 v[38:39], v123 offset:3328\n"
    "ds_read_b128 v[48:51], v115 offset:35776\n"
    "ds_read_b128 v[52:55], v119 offset:35776\n"
    "s_waitcnt lgkmcnt(4)\n"
    "v_mfma_f32_32x32x16_bf16 v[0:15], v[32:35], v[40:43], v[0:15]\n"
    "s_add_u32 m0, %[m0c], 2048\n"
    "s_nop 0\n"
    "buffer_load_dwordx4 v113, %[srd], s65 offen lds\n"
    "s_add_u32 s65, s65, 20480\n"
    "v_mfma_f32_32x32x16_bf16 v[16:31], v[32:35], v[44:47], v[16:31]\n"
    "s_waitcnt vmcnt(10)\n"
    "ds_read_b64_tr_b16 v[32:33], v123 offset:4096\n"
    "ds_read_b64_tr_b16 v[34:35], v123 offset:4352\n"
    "ds_read_b128 v[40:43], v116 offset:36800\n"
    "ds_read_b128 v[44:47], v120 offset:36800\n"
    "s_waitcnt lgkmcnt(4)\n"
    "v_mfma_f32_32x32x16_bf16 v[0:15], v[36:39], v[48:51], v[0:15]\n"
    "s_add_u32 m0, %[m0c], 3072\n"
    "s_nop 0\n"
    "buffer_load_dwordx4 v113, %[srd], s65 offen lds\n"
    "s_add_u32 s65, s65, 20480\n"
    "v_mfma_f32_32x32x16_bf16 v[16:31], v[36:39], v[52:55], v[16:31]\n"
    "s_waitcnt vmcnt(10)\n"
    "ds_read_b64_tr_b16 v[36:37], v123 offset:5120\n"
    "ds_read_b64_tr_b16 v[38:39], v123 offset:5376\n"
    "ds_read_b128 v[48:51], v117 offset:37824\n"
    "ds_read_b128 v[52:55], v121 offset:37824\n"
    "s_waitcnt lgkmcnt(4)\n"
    "v_mfma_f32_32x32x16_bf16 v[0:15], v[32:35], v[40:43], v[0:15]\n"
    "s_add_u32 m0, %[m0c], 4096\n"
    "s_nop 0\n"
    "buffer_load_dwordx4 v113, %[srd], s65 offen lds\n"
    "s_add_u32 s65, s65, 20480\n"
    "v_mfma_f32_32x32x16_bf16 v[16:31], v[32:35], v[44:47], v[16:31]\n"
    "s_waitcnt vmcnt(10)\n"
    "ds_read_b64_tr_b16 v[32:33], v122 offset:0\n"
    "ds_read_b64_tr_b16 v[34:35], v122 offset:256\n"
    "ds_read_b128 v[40:43], v114 offset:38848\n"
    "ds_read_b128 v[44:47], v118 offset:38848\n"
    "s_waitcnt lgkmcnt(4)\n"
    "v_mfma_f32_32x32x16_bf16 v[0:15], v[36:39], v[48:51], v[0:15]\n"
    "s_add_u32 m0, %[m0c], 5120\n"
    "s_nop 0\n"
    "buffer_load_dwordx4 v113, %[srd], s65 offen lds\n"
    "s_add_u32 s65, s65, 20480\n"
    "v_mfma_f32_32x32x16_bf16 v[16:31], v[36:39], v[52:55], v[16:31]\n"
    "s_waitcnt vmcnt(10)\n"
    "ds_read_b64_tr_b16 v[36:37], v122 offset:1024\n"
    "ds_read_b64_tr_b16 v[38:39], v122 offset:1280\n"
    "ds_read_b128 v[48:51], v115 offset:39872\n"
    "ds_read_b128 v[52:55], v119 offset:39872\n"
    "s_waitcnt lgkmcnt(4)\n"
    "s_mov_b64 exec, 1\n"
    "ds_add_u32 v89, v88 offset:60\n"
    "s_mov_b64 exec, -1\n"
    "v_mfma_f32_32x32x16_bf16 v[0:15], v[32:35], v[40:43], v[0:15]\n"
    "s_add_u32 m0, %[m0b], 0\n"
    "s_nop 0\n"
    "buffer_load_dwordx4 v113, %[srd], s65 offen lds\n"
    "s_add_u32 s65, s65, 20480\n"
    "v_mfma_f32_32x32x16_bf16 v[16:31], v[32:35], v[44:47], v[16:31]\n"
    "s_waitcnt vmcnt(10)\n"
    "ds_read_b64_tr_b16 v[32:33], v122 offset:2048\n"
    "ds_read_b64_tr_b16 v[34:35], v122 offset:2304\n"
    "ds_read_b128 v[40:43], v116 offset:40896\n"
    "ds_read_b128 v[44:47], v120 offset:40896\n"
    "s_waitcnt lgkmcnt(5)\n"
    "v_mfma_f32_32x32x16_bf16 v[0:15], v[36:39], v[48:51], v[0:15]\n"
    "s_add_u32 m0, %[m0b], 1024\n"
    "s_nop 0\n"
    "buffer_load_dwordx4 v113, %[srd], s65 offen lds\n"
    "s_add_u32 s65, s65, 20480\n"
    "v_mfma_f32_32x32x16_bf16 v[16:31], v[36:39], v[52:55], v[16:31]\n"
    "s_waitcnt vmcnt(10)\n"
    "ds_read_b64_tr_b16 v[36:37], v122 offset:3072\n"
    "ds_read_b64_tr_b16 v[38:39], v122 offset:3328\n"
    "ds_read_b128 v[48:51], v117 offset:41920\n"
    "ds_read_b128 v[52:55], v121 offset:41920\n"
    "s_waitcnt lgkmcnt(4)\n"
    "v_mfma_f32_32x32x16_bf16 v[0:15], v[32:35], v[40:43], v[0:15]\n"
    "s_add_u32 m0, %[m0b], 2048\n"
    "s_nop 0\n"
    "buffer_load_dwordx4 v113, %[srd], s65 offen lds\n"
    "s_add_u32 s65, s65, 20480\n"
    "v_mfma_f32_32x32x16_bf16 v[16:31], v[32:35], v[44:47], v[16:31]\n"
    "s_waitcnt vmcnt(10)\n"
    "ds_read_b64_tr_b16 v[32:33], v122 offset:4096\n"
    "ds_read_b64_tr_b16 v[34:35], v122 offset:4352\n"
    "ds_read_b128 v[40:43], v114 offset:42944\n"
    "ds_read_b128 v[44:47], v118 offset:42944\n"
    "s_waitcnt lgkmcnt(4)\n"
    "v_mfma_f32_32x32x16_bf16 v[0:15], v[36:39], v[48:51], v[0:15]\n"
    "s_add_u32 m0, %[m0b], 3072\n"
    "s_nop 0\n"
    "buffer_load_dwordx4 v113, %[srd], s65 offen lds\n"
    "s_add_u32 s65, s65, 20480\n"
    "v_mfma_f32_32x32x16_bf16 v[16:31], v[36:39], v[52:55], v[16:31]\n"
    "s_waitcnt vmcnt(10)\n"
    "ds_read_b64_tr_b16 v[36:37], v122 offset:5120\n"
    "ds_read_b64_tr_b16 v[38:39], v122 offset:5376\n"
    "ds_read_b128 v[48:51], v115 offset:43968\n"
    "ds_read_b128 v[52:55], v119 offset:43968\n"
    "s_waitcnt lgkmcnt(4)\n"
    "v_mfma_f32_32x32x16_bf16 v[0:15], v[32:35], v[40:43], v[0:15]\n"
    "v_mfma_f32_32x32x16_bf16 v[16:31], v[32:35], v[44:47], v[16:31]\n"
    "s_waitcnt vmcnt(9)\n"
    "ds_read_b64_tr_b16 v[32:33], v123 offset:0\n"
    "ds_read_b64_tr_b16 v[34:35], v123 offset:256\n"
    "ds_read_b128 v[40:43], v116 offset:44992\n"
    "ds_read_b128 v[44:47], v120 offset:44992\n"
    "s_waitcnt lgkmcnt(4)\n"
    "v_mfma_f32_32x32x16_bf16 v[0:15], v[36:39], v[48:51], v[0:15]\n"
    "v_mfma_f32_32x32x16_bf16 v[16:31], v[36:39], v[52:55], v[16:31]\n"
    "s_waitcnt vmcnt(8)\n"
    "ds_read_b64_tr_b16 v[36:37], v123 offset:1024\n"
    "ds_read_b64_tr_b16 v[38:39], v123 offset:1280\n"
    "ds_read_b128 v[48:51], v117 offset:46016\n"
    "ds_read_b128 v[52:55], v121 offset:46016\n"
    "s_waitcnt lgkmcnt(4)\n"
    "v_mfma_f32_32x32x16_bf16 v[0:15], v[32:35], v[40:43], v[0:15]\n"
    "v_mfma_f32_32x32x16_bf16 v[16:31], v[32:35], v[44:47], v[16:31]\n"
    "s_waitcnt vmcnt(7)\n"
    "ds_read_b64_tr_b16 v[32:33], v123 offset:2048\n"
    "ds_read_b64_tr_b16 v[34:35], v123 offset:2304\n"
    "ds_read_b128 v[40:43], v114 offset:47040\n"
    "ds_read_b128 v[44:47], v118 offset:47040\n"
    "s_waitcnt lgkmcnt(4)\n"
    "v_mfma_f32_32x32x16_bf16 v[0:15], v[36:39], v[48:51], v[0:15]\n"
    "v_mfma_f32_32x32x16_bf16 v[16:31], v[36:39], v[52:55], v[16:31]\n"
    "s_waitcnt vmcnt(6)\n"
    "ds_read_b64_tr_b16 v[36:37], v123 offset:3072\n"
    "ds_read_b64_tr_b16 v[38:39], v123 offset:3328\n"
    "ds_read_b128 v[48:51], v115 offset:48064\n"
    "ds_read_b128 v[52:55], v119 offset:48064\n"
    "s_waitcnt lgkmcnt(4)\n"
    "v_mfma_f32_32x32x16_bf16 v[0:15], v[32:35], v[40:43], v[0:15]\n"
    "v_mfma_f32_32x32x16_bf16 v[16:31], v[32:35], v[44:47], v[16:31]\n"
    "s_waitcnt vmcnt(5)\n"
    "ds_read_b64_tr_b16 v[32:33], v123 offset:4096\n"
    "ds_read_b64_tr_b16 v[34:35], v123 offset:4352\n"
    "ds_read_b128 v[40:43], v116 offset:49088\n"
    "ds_read_b128 v[44:47], v120 offset:49088\n"
    "s_waitcnt lgkmcnt(4)\n"
    "ds_read_b32 v66, v93\n"
    "v_mfma_f32_32x32x16_bf16 v[0:15], v[36:39], v[48:51], v[0:15]\n"
    "v_mfma_f32_32x32x16_bf16 v[16:31], v[36:39], v[52:55], v[16:31]\n"
    "s_waitcnt vmcnt(4)\n"
    "ds_read_b64_tr_b16 v[36:37], v123 offset:5120\n"
    "ds_read_b64_tr_b16 v[38:39], v123 offset:5376\n"
    "ds_read_b128 v[48:51], v117 offset:50112\n"
    "ds_read_b128 v[52:55], v121 offset:50112\n"
    "s_waitcnt lgkmcnt(5)\n"
    "v_mfma_f32_32x32x16_bf16 v[0:15], v[32:35], v[40:43], v[0:15]\n"
    "v_mfma_f32_32x32x16_bf16 v[16:31], v[32:35], v[44:47], v[16:31]\n"
    "s_waitcnt vmcnt(3)\n"
    "ds_read_b64_tr_b16 v[32:33], v122 offset:0\n"
    "ds_read_b64_tr_b16 v[34:35], v122 offset:256\n"
    "ds_read_b128 v[40:43], v114 offset:51136\n"
    "ds_read_b128 v[44:47], v118 offset:51136\n"
    "s_waitcnt lgkmcnt(4)\n"
    "s_waitcnt vmcnt(0)\n"
    "v_readfirstlane_b32 s59, v66\n"
    "s_nop 0\n"
    "s_cmp_eq_u32 s59, 0\n"
    "s_cbranch_scc1 EK%=\n"
    "s_mov_b64 s[60:61], %[kb]\n"
    "global_load_dwordx4 a[64:67], v84, s[60:61] sc1\n"
    "global_load_dwordx2 a[68:69], v85, s[60:61] sc1\n"
    "global_load_dwordx4 a[70:73], v86, s[60:61] sc1\n"
    "global_load_dwordx4 a[74:77], v86, s[60:61] offset:768 sc1\n"
    "s_add_u32 s60, s60, 2816\n"
    "s_addc_u32 s61, s61, 0\n"
    "global_load_dwordx4 a[78:81], v84, s[60:61] sc1\n"
    "global_load_dwordx4 a[84:87], v86, s[60:61] sc1\n"
    "global_load_dwordx4 a[88:91], v86, s[60:61] offset:768 sc1\n"
    "s_add_u32 s60, s60, 2816\n"
    "s_addc_u32 s61, s61, 0\n"
    "global_load_dwordx4 a[92:95], v84, s[60:61] sc1\n"
    "global_load_dwordx2 a[96:97], v85, s[60:61] sc1\n"
    "global_load_dwordx4 a[98:101], v86, s[60:61] sc1\n"
    "global_load_dwordx4 a[102:105], v86, s[60:61] offset:768 sc1\n"
    "s_add_u32 s60, s60, 2816\n"
    "s_addc_u32 s61, s61, 0\n"
    "global_load_dwordx2 v[100:101], v110, %[xp] offset:0\n"
    "global_load_dwordx2 v[102:103], v111, %[xp] offset:0\n"
    "global_load_dwordx2 v[104:105], v110, %[xp] offset:64\n"
    "global_load_dwordx2 v[106:107], v111, %[xp] offset:64\n"
    "global_load_dwordx2 v[108:109], v112, %[xp] offset:0\n"
    "s_mov_b64 exec, 1\n"
    "ds_add_u32 v89, v88 offset:12\n"
    "s_mov_b64 exec, -1\n"
    "EK%=:\n"
    "v_mfma_f32_32x32x16_bf16 v[0:15], v[36:39], v[48:51], v[0:15]\n"
    "v_mfma_f32_32x32x16_bf16 v[16:31], v[36:39], v[52:55], v[16:31]\n"
    "ds_read_b64_tr_b16 v[36:37], v122 offset:1024\n"
    "ds_read_b64_tr_b16 v[38:39], v122 offset:1280\n"
    "ds_read_b128 v[48:51], v115 offset:52160\n"
    "ds_read_b128 v[52:55], v119 offset:52160\n"
    "s_waitcnt lgkmcnt(4)\n"
    "v_mfma_f32_32x32x16_bf16 v[0:15], v[32:35], v[40:43], v[0:15]\n"
    "v_mfma_f32_32x32x16_bf16 v[16:31], v[32:35], v[44:47], v[16:31]\n"
    "ds_read_b64_tr_b16 v[32:33], v122 offset:2048\n"
    "ds_read_b64_tr_b16 v[34:35], v122 offset:2304\n"
    "ds_read_b128 v[40:43], v116 offset:53184\n"
    "ds_read_b128 v[44:47], v120 offset:53184\n"
    "s_waitcnt lgkmcnt(4)\n"
    "v_mfma_f32_32x32x16_bf16 v[0:15], v[36:39], v[48:51], v[0:15]\n"
    "v_mfma_f32_32x32x16_bf16 v[16:31], v[36:39], v[52:55], v[16:31]\n"
    "ds_read_b64_tr_b16 v[36:37], v122 offset:3072\n"
    "ds_read_b64_tr_b16 v[38:39], v122 offset:3328\n"
    "ds_read_b128 v[48:51], v117 offset:54208\n"
    "ds_read_b128 v[52:55], v121 offset:54208\n"
    "s_waitcnt lgkmcnt(4)\n"
    "v_mfma_f32_32x32x16_bf16 v[0:15], v[32:35], v[40:43], v[0:15]\n"
    "v_mfma_f32_32x32x16_bf16 v[16:31], v[32:35], v[44:47], v[16:31]\n"
    "s_waitcnt lgkmcnt(0)\n"
    "v_mfma_f32_32x32x16_bf16 v[0:15], v[36:39], v[48:51], v[0:15]\n"
    "v_mfma_f32_32x32x16_bf16 v[16:31], v[36:39], v[52:55], v[16:31]\n"
    "s_mov_b32 m0, s64\n"
    "s_cmp_lg_u32 s59, 0\n"
    "s_cbranch_scc1 EL%=\n"
    "s_mov_b32 s62, 0x100000\n"
    "KRS%=:\n"
    "ds_read_b32 v32, v93 offset:0\n"
    "s_sub_u32 s62, s62, 1\n"
    "s_cmp_eq_u32 s62, 0\n"
    "s_cbranch_scc1 KRX%=\n"
    "s_waitcnt lgkmcnt(0)\n"
    "v_readfirstlane_b32 s63, v32\n"
    "s_nop 0\n"
    "s_cmp_lt_u32 s63, 1\n"
    "s_cbranch_scc1 KRS%=\n"
    "KRX%=:\n"
    "s_waitcnt lgkmcnt(0)\n"
    "s_mov_b64 s[60:61], %[kb]\n"
    "global_load_dwordx4 a[64:67], v84, s[60:61] sc1\n"
    "global_load_dwordx2 a[68:69], v85, s[60:61] sc1\n"
    "global_load_dwordx4 a[70:73], v86, s[60:61] sc1\n"
    "global_load_dwordx4 a[74:77], v86, s[60:61] offset:768 sc1\n"
    "s_add_u32 s60, s60, 2816\n"
    "s_addc_u32 s61, s61, 0\n"
    "global_load_dwordx4 a[78:81], v84, s[60:61] sc1\n"
    "global_load_dwordx4 a[84:87], v86, s[60:61] sc1\n"
    "global_load_dwordx4 a[88:91], v86, s[60:61] offset:768 sc1\n"
    "s_add_u32 s60, s60, 2816\n"
    "s_addc_u32 s61, s61, 0\n"
    "global_load_dwordx4 a[92:95], v84, s[60:61] sc1\n"
    "global_load_dwordx2 a[96:97], v85, s[60:61] sc1\n"
    "global_load_dwordx4 a[98:101], v86, s[60:61] sc1\n"
    "global_load_dwordx4 a[102:105], v86, s[60:61] offset:768 sc1\n"
    "s_add_u32 s60, s60, 2816\n"
    "s_addc_u32 s61, s61, 0\n"
    "global_load_dwordx2 v[100:101], v110, %[xp] offset:0\n"
    "global_load_dwordx2 v[102:103], v111, %[xp] offset:0\n"
    "global_load_dwordx2 v[104:105], v110, %[xp] offset:64\n"
    "global_load_dwordx2 v[106:107], v111, %[xp] offset:64\n"
    "global_load_dwordx2 v[108:109], v112, %[xp] offset:0\n"
    "s_mov_b64 exec, 1\n"
    "ds_add_u32 v89, v88 offset:12\n"
    "s_mov_b64 exec, -1\n"
    "EL%=:\n"
    "v_mul_f32 v36, 0x3ea52b5b, v90\n"
    "v_mul_f32 v37, 0x3ea52b5b, v90\n"
    "v_mul_f32 v38, 0x3ea52b5b, v91\n"
    "v_mul_f32 v39, 0x3ea52b5b, v91\n"
    "v_pk_mul_f32 v[0:1], v[0:1], v[36:37]\n"
    "v_pk_mul_f32 v[16:17], v[16:17], v[38:39]\n"
    "v_pk_mul_f32 v[2:3], v[2:3], v[36:37]\n"
    "v_pk_mul_f32 v[18:19], v[18:19], v[38:39]\n"
    "v_pk_mul_f32 v[4:5], v[4:5], v[36:37]\n"
    "v_pk_mul_f32 v[20:21], v[20:21], v[38:39]\n"
    "v_pk_mul_f32 v[6:7], v[6:7], v[36:37]\n"
    "v_pk_mul_f32 v[22:23], v[22:23], v[38:39]\n"
    "v_pk_mul_f32 v[8:9], v[8:9], v[36:37]\n"
    "v_pk_mul_f32 v[24:25], v[24:25], v[38:39]\n"
    "v_pk_mul_f32 v[10:11], v[10:11], v[36:37]\n"
    "v_pk_mul_f32 v[26:27], v[26:27], v[38:39]\n"
    "v_cvt_pk_bf16_f32 v64, v0, v1\n"
    "v_cvt_pk_bf16_f32 v68, v16, v17\n"
    "v_cvt_pk_bf16_f32 v65, v2, v3\n"
    "v_cvt_pk_bf16_f32 v69, v18, v19\n"
    "v_cvt_pk_bf16_f32 v66, v4, v5\n"
    "v_cvt_pk_bf16_f32 v70, v20, v21\n"
    "v_cvt_pk_bf16_f32 v67, v6, v7\n"
    "v_cvt_pk_bf16_f32 v71, v22, v23\n"
    "v_cvt_pk_bf16_f32 v80, v8, v9\n"
    "v_cvt_pk_bf16_f32 v81, v10, v11\n"
    "v_cvt_pk_bf16_f32 v34, v24, v25\n"
    "v_cvt_pk_bf16_f32 v35, v26, v27\n"
    "ds_write_b128 v94, v[64:67]\n"
    "ds_write_b128 v94, v[68:71] offset:1024\n"
    "v_permlane32_swap_b32 v80, v34\n"
    "v_permlane32_swap_b32 v81, v35\n"
    "s_nop 0\n"
    "ds_write_b64 v94, v[80:81] offset:2048\n"
    "s_waitcnt lgkmcnt(0)\n"
    "s_mov_b64 exec, 1\n"
    "ds_write_b32 v95, v88\n"
    "ds_add_u32 v89, v88 offset:4\n"
    "s_mov_b64 exec, -1\n"
    "v_mov_b32 v56, 0\n"
    "v_mov_b32 v57, 0\n"
    "v_mov_b32 v58, 0\n"
    "v_mov_b32 v59, 0\n"
    "v_mov_b32 v60, 0\n"
    "v_mov_b32 v61, 0\n"
    "v_mov_b32 v62, 0\n"
    "v_mov_b32 v63, 0\n"
    "s_mov_b32 s62, 7\n"
    "s_waitcnt vmcnt(12)\n"
    "v_mfma_f32_32x32x4_2b_bf16 v[0:31], a[68:69], v[80:81], 0 cbsz:1 abid:0\n"
    "s_nop 15\n"
    "s_nop 1\n"
    "v_mfma_f32_32x32x16_bf16 v[0:15], a[64:67], v[64:67], v[0:15]\n"
    "v_mfma_f32_32x32x16_bf16 v[16:31], a[64:67], v[68:71], v[16:31]\n"
    "s_mov_b32 s65, 0x100000\n"
    "QXS%=:\n"
    "ds_read_b32 v32, v96 offset:0\n"
    "s_sub_u32 s65, s65, 1\n"
    "s_cmp_eq_u32 s65, 0\n"
    "s_cbranch_scc1 QXX%=\n"
    "s_waitcnt lgkmcnt(0)\n"
    "v_readfirstlane_b32 s63, v32\n"
    "s_nop 0\n"
    "s_cmp_lt_u32 s63, 1\n"
    "s_cbranch_scc1 QXS%=\n"
    "QXX%=:\n"
    "s_waitcnt lgkmcnt(0)\n"
    "ds_read_b128 v[72:75], v87\n"
    "ds_read_b128 v[76:79], v87 offset:1024\n"
    "ds_read_b64 v[82:83], v87 offset:2048\n"
    "s_waitcnt lgkmcnt(0)\n"
    "s_nop 11\n"
    "ATT%=_loop:\n"
    "v_exp_f32 v0, v0\n"
    "v_exp_f32 v1, v1\n"
    "v_cvt_pk_bf16_f32 v59, v62, v63\n"
    "v_exp_f32 v2, v2\n"
    "v_exp_f32 v3, v3\n"
    "v_mfma_f32_32x32x16_bf16 a[48:63], a[116:119], v[56:59], a[48:63]\n"
    "global_load_dwordx4 a[106:109], v84, s[60:61] sc1\n"
    "v_cvt_pk_bf16_f32 v0, v0, v1\n"
    "v_exp_f32 v4, v4\n"
    "v_exp_f32 v5, v5\n"
    "v_cvt_pk_bf16_f32 v1, v2, v3\n"
    "v_exp_f32 v6, v6\n"
    "v_exp_f32 v7, v7\n"
    "v_mfma_f32_32x32x4_2b_bf16 v[32:63], a[68:69], v[82:83], 0 cbsz:1 abid:0\n"
    "v_cvt_pk_bf16_f32 v2, v4, v5\n"
    "v_exp_f32 v8, v8\n"
    "v_exp_f32 v9, v9\n"
    "v_cvt_pk_bf16_f32 v3, v6, v7\n"
    "v_exp_f32 v10, v10\n"
    "v_exp_f32 v11, v11\n"
    "v_cvt_pk_bf16_f32 v8, v8, v9\n"
    "v_exp_f32 v12, v12\n"
    "v_exp_f32 v13, v13\n"
    "v_mfma_f32_32x32x16_bf16 a[0:15], a[70:73], v[0:3], a[0:15]\n"
    "global_load_dwordx4 a[112:115], v86, s[60:61] sc1\n"
    "v_cvt_pk_bf16_f32 v9, v10, v11\n"
    "v_exp_f32 v14, v14\n"
    "v_exp_f32 v15, v15\n"
    "v_cvt_pk_bf16_f32 v10, v12, v13\n"
    "v_exp_f32 v16, v16\n"
    "v_exp_f32 v17, v17\n"
    "v_cvt_pk_bf16_f32 v11, v14, v15\n"
    "v_exp_f32 v18, v18\n"
    "v_exp_f32 v19, v19\n"
    "v_mfma_f32_32x32x16_bf16 v[32:47], a[64:67], v[72:75], v[32:47]\n"
    "global_load_dwordx4 a[116:119], v86, s[60:61] offset:768 sc1\n"
    "v_cvt_pk_bf16_f32 v16, v16, v17\n"
    "v_exp_f32 v20, v20\n"
    "v_exp_f32 v21, v21\n"
    "v_cvt_pk_bf16_f32 v17, v18, v19\n"
    "v_exp_f32 v22, v22\n"
    "v_exp_f32 v23, v23\n"
    "v_mfma_f32_32x32x16_bf16 a[0:15], a[74:77], v[8:11], a[0:15]\n"
    "s_add_u32 s60, s60, 2816\n"
    "s_addc_u32 s61, s61, 0\n"
    "v_cvt_pk_bf16_f32 v18, v20, v21\n"
    "v_exp_f32 v24, v24\n"
    "v_exp_f32 v25, v25\n"
    "v_cvt_pk_bf16_f32 v19, v22, v23\n"
    "v_exp_f32 v26, v26\n"
    "v_exp_f32 v27, v27\n"
    "v_mfma_f32_32x32x16_bf16 v[48:63], a[64:67], v[76:79], v[48:63]\n"
    "v_cvt_pk_bf16_f32 v24, v24, v25\n"
    "v_exp_f32 v28, v28\n"
    "v_exp_f32 v29, v29\n"
    "v_cvt_pk_bf16_f32 v25, v26, v27\n"
    "v_exp_f32 v30, v30\n"
    "v_exp_f32 v31, v31\n"
    "v_mfma_f32_32x32x16_bf16 a[16:31], a[70:73], v[16:19], a[16:31]\n"
    "v_cvt_pk_bf16_f32 v26, v28, v29\n"
    "v_exp_f32 v32, v32\n"
    "v_exp_f32 v33, v33\n"
    "v_cvt_pk_bf16_f32 v27, v30, v31\n"
    "v_exp_f32 v34, v34\n"
    "v_exp_f32 v35, v35\n"
    "v_mfma_f32_32x32x16_bf16 a[16:31], a[74:77], v[24:27], a[16:31]\n"
    "v_cvt_pk_bf16_f32 v32, v32, v33\n"
    "v_exp_f32 v36, v36\n"
    "v_exp_f32 v37, v37\n"
    "v_cvt_pk_bf16_f32 v33, v34, v35\n"
    "v_exp_f32 v38, v38\n"
    "v_exp_f32 v39, v39\n"
    "s_waitcnt vmcnt(7)\n"
    "v_mfma_f32_32x32x4_2b_bf16 v[0:31], a[68:69], v[80:81], 0 cbsz:1 abid:1\n"
    "v_cvt_pk_bf16_f32 v34, v36, v37\n"
    "v_exp_f32 v40, v40\n"
    "v_exp_f32 v41, v41\n"
    "v_cvt_pk_bf16_f32 v35, v38, v39\n"
    "v_exp_f32 v42, v42\n"
    "v_exp_f32 v43, v43\n"
    "v_cvt_pk_bf16_f32 v40, v40, v41\n"
    "v_exp_f32 v44, v44\n"
    "v_exp_f32 v45, v45\n"
    "v_mfma_f32_32x32x16_bf16 a[32:47], a[70:73], v[32:35], a[32:47]\n"
    "v_cvt_pk_bf16_f32 v41, v42, v43\n"
    "v_exp_f32 v46, v46\n"
    "v_exp_f32 v47, v47\n"
    "v_cvt_pk_bf16_f32 v42, v44, v45\n"
    "v_exp_f32 v48, v48\n"
    "v_exp_f32 v49, v49\n"
    "v_cvt_pk_bf16_f32 v43, v46, v47\n"
    "v_exp_f32 v50, v50\n"
    "v_exp_f32 v51, v51\n"
    "v_mfma_f32_32x32x16_bf16 v[0:15], a[78:81], v[64:67], v[0:15]\n"
    "v_cvt_pk_bf16_f32 v48, v48, v49\n"
    "v_exp_f32 v52, v52\n"
    "v_exp_f32 v53, v53\n"
    "v_cvt_pk_bf16_f32 v49, v50, v51\n"
    "v_exp_f32 v54, v54\n"
    "v_exp_f32 v55, v55\n"
    "v_mfma_f32_32x32x16_bf16 a[32:47], a[74:77], v[40:43], a[32:47]\n"
    "v_cvt_pk_bf16_f32 v50, v52, v53\n"
    "v_exp_f32 v56, v56\n"
    "v_exp_f32 v57, v57\n"
    "v_cvt_pk_bf16_f32 v51, v54, v55\n"
    "v_exp_f32 v58, v58\n"
    "v_exp_f32 v59, v59\n"
    "v_mfma_f32_32x32x16_bf16 v[16:31], a[78:81], v[68:71], v[16:31]\n"
    "v_cvt_pk_bf16_f32 v56, v56, v57\n"
    "v_exp_f32 v60, v60\n"
    "v_exp_f32 v61, v61\n"
    "v_cvt_pk_bf16_f32 v57, v58, v59\n"
    "v_exp_f32 v62, v62\n"
    "v_exp_f32 v63, v63\n"
    "v_mfma_f32_32x32x16_bf16 a[48:63], a[70:73], v[48:51], a[48:63]\n"
    "v_cvt_pk_bf16_f32 v58, v60, v61\n"
    "v_exp_f32 v0, v0\n"
    "v_exp_f32 v1, v1\n"
    "v_cvt_pk_bf16_f32 v59, v62, v63\n"
    "v_exp_f32 v2, v2\n"
    "v_exp_f32 v3, v3\n"
    "v_mfma_f32_32x32x16_bf16 a[48:63], a[74:77], v[56:59], a[48:63]\n"
    "global_load_dwordx4 a[64:67], v84, s[60:61] sc1\n"
    "v_cvt_pk_bf16_f32 v0, v0, v1\n"
    "v_exp_f32 v4, v4\n"
    "v_exp_f32 v5, v5\n"
    "v_cvt_pk_bf16_f32 v1, v2, v3\n"
    "v_exp_f32 v6, v6\n"
    "v_exp_f32 v7, v7\n"
    "v_mfma_f32_32x32x4_2b_bf16 v[32:63], a[68:69], v[82:83], 0 cbsz:1 abid:1\n"
    "global_load_dwordx2 a[68:69], v85, s[60:61] sc1\n"
    "v_cvt_pk_bf16_f32 v2, v4, v5\n"
    "v_exp_f32 v8, v8\n"
    "v_exp_f32 v9, v9\n"
    "v_cvt_pk_bf16_f32 v3, v6, v7\n"
    "v_exp_f32 v10, v10\n"
    "v_exp_f32 v11, v11\n"
    "v_cvt_pk_bf16_f32 v8, v8, v9\n"
    "v_exp_f32 v12, v12\n"
    "v_exp_f32 v13, v13\n"
    "v_mfma_f32_32x32x16_bf16 a[0:15], a[84:87], v[0:3], a[0:15]\n"
    "global_load_dwordx4 a[70:73], v86, s[60:61] sc1\n"
    "v_cvt_pk_bf16_f32 v9, v10, v11\n"
    "v_exp_f32 v14, v14\n"
    "v_exp_f32 v15, v15\n"
    "v_cvt_pk_bf16_f32 v10, v12, v13\n"
    "v_exp_f32 v16, v16\n"
    "v_exp_f32 v17, v17\n"
    "v_cvt_pk_bf16_f32 v11, v14, v15\n"
    "v_exp_f32 v18, v18\n"
    "v_exp_f32 v19, v19\n"
    "v_mfma_f32_32x32x16_bf16 v[32:47], a[78:81], v[72:75], v[32:47]\n"
    "global_load_dwordx4 a[74:77], v86, s[60:61] offset:768 sc1\n"
    "v_cvt_pk_bf16_f32 v16, v16, v17\n"
    "v_exp_f32 v20, v20\n"
    "v_exp_f32 v21, v21\n"
    "v_cvt_pk_bf16_f32 v17, v18, v19\n"
    "v_exp_f32 v22, v22\n"
    "v_exp_f32 v23, v23\n"
    "v_mfma_f32_32x32x16_bf16 a[0:15], a[88:91], v[8:11], a[0:15]\n"
    "s_add_u32 s60, s60, 2816\n"
    "s_addc_u32 s61, s61, 0\n"
    "v_cvt_pk_bf16_f32 v18, v20, v21\n"
    "v_exp_f32 v24, v24\n"
    "v_exp_f32 v25, v25\n"
    "v_cvt_pk_bf16_f32 v19, v22, v23\n"
    "v_exp_f32 v26, v26\n"
    "v_exp_f32 v27, v27\n"
    "v_mfma_f32_32x32x16_bf16 v[48:63], a[78:81], v[76:79], v[48:63]\n"
    "v_cvt_pk_bf16_f32 v24, v24, v25\n"
    "v_exp_f32 v28, v28\n"
    "v_exp_f32 v29, v29\n"
    "v_cvt_pk_bf16_f32 v25, v26, v27\n"
    "v_exp_f32 v30, v30\n"
    "v_exp_f32 v31, v31\n"
    "v_mfma_f32_32x32x16_bf16 a[16:31], a[84:87], v[16:19], a[16:31]\n"
    "v_cvt_pk_bf16_f32 v26, v28, v29\n"
    "v_exp_f32 v32, v32\n"
    "v_exp_f32 v33, v33\n"
    "v_cvt_pk_bf16_f32 v27, v30, v31\n"
    "v_exp_f32 v34, v34\n"
    "v_exp_f32 v35, v35\n"
    "v_mfma_f32_32x32x16_bf16 a[16:31], a[88:91], v[24:27], a[16:31]\n"
    "v_cvt_pk_bf16_f32 v32, v32, v33\n"
    "v_exp_f32 v36, v36\n"
    "v_exp_f32 v37, v37\n"
    "v_cvt_pk_bf16_f32 v33, v34, v35\n"
    "v_exp_f32 v38, v38\n"
    "v_exp_f32 v39, v39\n"
    "s_waitcnt vmcnt(7)\n"
    "v_mfma_f32_32x32x4_2b_bf16 v[0:31], a[96:97], v[80:81], 0 cbsz:1 abid:0\n"
    "v_cvt_pk_bf16_f32 v34, v36, v37\n"
    "v_exp_f32 v40, v40\n"
    "v_exp_f32 v41, v41\n"
    "v_cvt_pk_bf16_f32 v35, v38, v39\n"
    "v_exp_f32 v42, v42\n"
    "v_exp_f32 v43, v43\n"
    "v_cvt_pk_bf16_f32 v40, v40, v41\n"
    "v_exp_f32 v44, v44\n"
    "v_exp_f32 v45, v45\n"
    "v_mfma_f32_32x32x16_bf16 a[32:47], a[84:87], v[32:35], a[32:47]\n"
    "v_cvt_pk_bf16_f32 v41, v42, v43\n"
    "v_exp_f32 v46, v46\n"
    "v_exp_f32 v47, v47\n"
    "v_cvt_pk_bf16_f32 v42, v44, v45\n"
    "v_exp_f32 v48, v48\n"
    "v_exp_f32 v49, v49\n"
    "v_cvt_pk_bf16_f32 v43, v46, v47\n"
    "v_exp_f32 v50, v50\n"
    "v_exp_f32 v51, v51\n"
    "v_mfma_f32_32x32x16_bf16 v[0:15], a[92:95], v[64:67], v[0:15]\n"
    "v_cvt_pk_bf16_f32 v48, v48, v49\n"
    "v_exp_f32 v52, v52\n"
    "v_exp_f32 v53, v53\n"
    "v_cvt_pk_bf16_f32 v49, v50, v51\n"
    "v_exp_f32 v54, v54\n"
    "v_exp_f32 v55, v55\n"
    "v_mfma_f32_32x32x16_bf16 a[32:47], a[88:91], v[40:43], a[32:47]\n"
    "v_cvt_pk_bf16_f32 v50, v52, v53\n"
    "v_exp_f32 v56, v56\n"
    "v_exp_f32 v57, v57\n"
    "v_cvt_pk_bf16_f32 v51, v54, v55\n"
    "v_exp_f32 v58, v58\n"
    "v_exp_f32 v59, v59\n"
    "v_mfma_f32_32x32x16_bf16 v[16:31], a[92:95], v[68:71], v[16:31]\n"
    "v_cvt_pk_bf16_f32 v56, v56, v57\n"
    "v_exp_f32 v60, v60\n"
    "v_exp_f32 v61, v61\n"
    "v_cvt_pk_bf16_f32 v57, v58, v59\n"
    "v_exp_f32 v62, v62\n"
    "v_exp_f32 v63, v63\n"
    "v_mfma_f32_32x32x16_bf16 a[48:63], a[84:87], v[48:51], a[48:63]\n"
    "v_cvt_pk_bf16_f32 v58, v60, v61\n"
    "v_exp_f32 v0, v0\n"
    "v_exp_f32 v1, v1\n"
    "v_cvt_pk_bf16_f32 v59, v62, v63\n"
    "v_exp_f32 v2, v2\n"
    "v_exp_f32 v3, v3\n"
    "v_mfma_f32_32x32x16_bf16 a[48:63], a[88:91], v[56:59], a[48:63]\n"
    "global_load_dwordx4 a[78:81], v84, s[60:61] sc1\n"
    "v_cvt_pk_bf16_f32 v0, v0, v1\n"
    "v_exp_f32 v4, v4\n"
    "v_exp_f32 v5, v5\n"
    "v_cvt_pk_bf16_f32 v1, v2, v3\n"
    "v_exp_f32 v6, v6\n"
    "v_exp_f32 v7, v7\n"
    "v_mfma_f32_32x32x4_2b_bf16 v[32:63], a[96:97], v[82:83], 0 cbsz:1 abid:0\n"
    "v_cvt_pk_bf16_f32 v2, v4, v5\n"
    "v_exp_f32 v8, v8\n"
    "v_exp_f32 v9, v9\n"
    "v_cvt_pk_bf16_f32 v3, v6, v7\n"
    "v_exp_f32 v10, v10\n"
    "v_exp_f32 v11, v11\n"
    "v_cvt_pk_bf16_f32 v8, v8, v9\n"
    "v_exp_f32 v12, v12\n"
    "v_exp_f32 v13, v13\n"
    "v_mfma_f32_32x32x16_bf16 a[0:15], a[98:101], v[0:3], a[0:15]\n"
    "global_load_dwordx4 a[84:87], v86, s[60:61] sc1\n"
    "v_cvt_pk_bf16_f32 v9, v10, v11\n"
    "v_exp_f32 v14, v14\n"
    "v_exp_f32 v15, v15\n"
    "v_cvt_pk_bf16_f32 v10, v12, v13\n"
    "v_exp_f32 v16, v16\n"
    "v_exp_f32 v17, v17\n"
    "v_cvt_pk_bf16_f32 v11, v14, v15\n"
    "v_exp_f32 v18, v18\n"
    "v_exp_f32 v19, v19\n"
    "v_mfma_f32_32x32x16_bf16 v[32:47], a[92:95], v[72:75], v[32:47]\n"
    "global_load_dwordx4 a[88:91], v86, s[60:61] offset:768 sc1\n"
    "v_cvt_pk_bf16_f32 v16, v16, v17\n"
    "v_exp_f32 v20, v20\n"
    "v_exp_f32 v21, v21\n"
    "v_cvt_pk_bf16_f32 v17, v18, v19\n"
    "v_exp_f32 v22, v22\n"
    "v_exp_f32 v23, v23\n"
    "v_mfma_f32_32x32x16_bf16 a[0:15], a[102:105], v[8:11], a[0:15]\n"
    "s_add_u32 s60, s60, 2816\n"
    "s_addc_u32 s61, s61, 0\n"
    "v_cvt_pk_bf16_f32 v18, v20, v21\n"
    "v_exp_f32 v24, v24\n"
    "v_exp_f32 v25, v25\n"
    "v_cvt_pk_bf16_f32 v19, v22, v23\n"
    "v_exp_f32 v26, v26\n"
    "v_exp_f32 v27, v27\n"
    "v_mfma_f32_32x32x16_bf16 v[48:63], a[92:95], v[76:79], v[48:63]\n"
    "v_cvt_pk_bf16_f32 v24, v24, v25\n"
    "v_exp_f32 v28, v28\n"
    "v_exp_f32 v29, v29\n"
    "v_cvt_pk_bf16_f32 v25, v26, v27\n"
    "v_exp_f32 v30, v30\n"
    "v_exp_f32 v31, v31\n"
    "v_mfma_f32_32x32x16_bf16 a[16:31], a[98:101], v[16:19], a[16:31]\n"
    "v_cvt_pk_bf16_f32 v26, v28, v29\n"
    "v_exp_f32 v32, v32\n"
    "v_exp_f32 v33, v33\n"
    "v_cvt_pk_bf16_f32 v27, v30, v31\n"
    "v_exp_f32 v34, v34\n"
    "v_exp_f32 v35, v35\n"
    "v_mfma_f32_32x32x16_bf16 a[16:31], a[102:105], v[24:27], a[16:31]\n"
    "v_cvt_pk_bf16_f32 v32, v32, v33\n"
    "v_exp_f32 v36, v36\n"
    "v_exp_f32 v37, v37\n"
    "v_cvt_pk_bf16_f32 v33, v34, v35\n"
    "v_exp_f32 v38, v38\n"
    "v_exp_f32 v39, v39\n"
    "s_waitcnt vmcnt(7)\n"
    "v_mfma_f32_32x32x4_2b_bf16 v[0:31], a[96:97], v[80:81], 0 cbsz:1 abid:1\n"
    "v_cvt_pk_bf16_f32 v34, v36, v37\n"
    "v_exp_f32 v40, v40\n"
    "v_exp_f32 v41, v41\n"
    "v_cvt_pk_bf16_f32 v35, v38, v39\n"
    "v_exp_f32 v42, v42\n"
    "v_exp_f32 v43, v43\n"
    "v_cvt_pk_bf16_f32 v40, v40, v41\n"
    "v_exp_f32 v44, v44\n"
    "v_exp_f32 v45, v45\n"
    "v_mfma_f32_32x32x16_bf16 a[32:47], a[98:101], v[32:35], a[32:47]\n"
    "v_cvt_pk_bf16_f32 v41, v42, v43\n"
    "v_exp_f32 v46, v46\n"
    "v_exp_f32 v47, v47\n"
    "v_cvt_pk_bf16_f32 v42, v44, v45\n"
    "v_exp_f32 v48, v48\n"
    "v_exp_f32 v49, v49\n"
    "v_cvt_pk_bf16_f32 v43, v46, v47\n"
    "v_exp_f32 v50, v50\n"
    "v_exp_f32 v51, v51\n"
    "v_mfma_f32_32x32x16_bf16 v[0:15], a[106:109], v[64:67], v[0:15]\n"
    "v_cvt_pk_bf16_f32 v48, v48, v49\n"
    "v_exp_f32 v52, v52\n"
    "v_exp_f32 v53, v53\n"
    "v_cvt_pk_bf16_f32 v49, v50, v51\n"
    "v_exp_f32 v54, v54\n"
    "v_exp_f32 v55, v55\n"
    "v_mfma_f32_32x32x16_bf16 a[32:47], a[102:105], v[40:43], a[32:47]\n"
    "v_cvt_pk_bf16_f32 v50, v52, v53\n"
    "v_exp_f32 v56, v56\n"
    "v_exp_f32 v57, v57\n"
    "v_cvt_pk_bf16_f32 v51, v54, v55\n"
    "v_exp_f32 v58, v58\n"
    "v_exp_f32 v59, v59\n"
    "v_mfma_f32_32x32x16_bf16 v[16:31], a[106:109], v[68:71], v[16:31]\n"
    "v_cvt_pk_bf16_f32 v56, v56, v57\n"
    "v_exp_f32 v60, v60\n"
    "v_exp_f32 v61, v61\n"
    "v_cvt_pk_bf16_f32 v57, v58, v59\n"
    "v_exp_f32 v62, v62\n"
    "v_exp_f32 v63, v63\n"
    "v_mfma_f32_32x32x16_bf16 a[48:63], a[98:101], v[48:51], a[48:63]\n"
    "v_cvt_pk_bf16_f32 v58, v60, v61\n"
    "v_exp_f32 v0, v0\n"
    "v_exp_f32 v1, v1\n"
    "v_cvt_pk_bf16_f32 v59, v62, v63\n"
    "v_exp_f32 v2, v2\n"
    "v_exp_f32 v3, v3\n"
    "v_mfma_f32_32x32x16_bf16 a[48:63], a[102:105], v[56:59], a[48:63]\n"
    "global_load_dwordx4 a[92:95], v84, s[60:61] sc1\n"
    "v_cvt_pk_bf16_f32 v0, v0, v1\n"
    "v_exp_f32 v4, v4\n"
    "v_exp_f32 v5, v5\n"
    "v_cvt_pk_bf16_f32 v1, v2, v3\n"
    "v_exp_f32 v6, v6\n"
    "v_exp_f32 v7, v7\n"
    "v_mfma_f32_32x32x4_2b_bf16 v[32:63], a[96:97], v[82:83], 0 cbsz:1 abid:1\n"
    "global_load_dwordx2 a[96:97], v85, s[60:61] sc1\n"
    "v_cvt_pk_bf16_f32 v2, v4, v5\n"
    "v_exp_f32 v8, v8\n"
    "v_exp_f32 v9, v9\n"
    "v_cvt_pk_bf16_f32 v3, v6, v7\n"
    "v_exp_f32 v10, v10\n"
    "v_exp_f32 v11, v11\n"
    "v_cvt_pk_bf16_f32 v8, v8, v9\n"
    "v_exp_f32 v12, v12\n"
    "v_exp_f32 v13, v13\n"
    "v_mfma_f32_32x32x16_bf16 a[0:15], a[112:115], v[0:3], a[0:15]\n"
    "global_load_dwordx4 a[98:101], v86, s[60:61] sc1\n"
    "v_cvt_pk_bf16_f32 v9, v10, v11\n"
    "v_exp_f32 v14, v14\n"
    "v_exp_f32 v15, v15\n"
    "v_cvt_pk_bf16_f32 v10, v12, v13\n"
    "v_exp_f32 v16, v16\n"
    "v_exp_f32 v17, v17\n"
    "v_cvt_pk_bf16_f32 v11, v14, v15\n"
    "v_exp_f32 v18, v18\n"
    "v_exp_f32 v19, v19\n"
    "v_mfma_f32_32x32x16_bf16 v[32:47], a[106:109], v[72:75], v[32:47]\n"
    "global_load_dwordx4 a[102:105], v86, s[60:61] offset:768 sc1\n"
    "v_cvt_pk_bf16_f32 v16, v16, v17\n"
    "v_exp_f32 v20, v20\n"
    "v_exp_f32 v21, v21\n"
    "v_cvt_pk_bf16_f32 v17, v18, v19\n"
    "v_exp_f32 v22, v22\n"
    "v_exp_f32 v23, v23\n"
    "v_mfma_f32_32x32x16_bf16 a[0:15], a[116:119], v[8:11], a[0:15]\n"
    "s_add_u32 s60, s60, 2816\n"
    "s_addc_u32 s61, s61, 0\n"
    "v_cvt_pk_bf16_f32 v18, v20, v21\n"
    "v_exp_f32 v24, v24\n"
    "v_exp_f32 v25, v25\n"
    "v_cvt_pk_bf16_f32 v19, v22, v23\n"
    "v_exp_f32 v26, v26\n"
    "v_exp_f32 v27, v27\n"
    "v_mfma_f32_32x32x16_bf16 v[48:63], a[106:109], v[76:79], v[48:63]\n"
    "v_cvt_pk_bf16_f32 v24, v24, v25\n"
    "v_exp_f32 v28, v28\n"
    "v_exp_f32 v29, v29\n"
    "v_cvt_pk_bf16_f32 v25, v26, v27\n"
    "v_exp_f32 v30, v30\n"
    "v_exp_f32 v31, v31\n"
    "v_mfma_f32_32x32x16_bf16 a[16:31], a[112:115], v[16:19], a[16:31]\n"
    "v_cvt_pk_bf16_f32 v26, v28, v29\n"
    "v_exp_f32 v32, v32\n"
    "v_exp_f32 v33, v33\n"
    "v_cvt_pk_bf16_f32 v27, v30, v31\n"
    "v_exp_f32 v34, v34\n"
    "v_exp_f32 v35, v35\n"
    "v_mfma_f32_32x32x16_bf16 a[16:31], a[116:119], v[24:27], a[16:31]\n"
    "v_cvt_pk_bf16_f32 v32, v32, v33\n"
    "v_exp_f32 v36, v36\n"
    "v_exp_f32 v37, v37\n"
    "v_cvt_pk_bf16_f32 v33, v34, v35\n"
    "v_exp_f32 v38, v38\n"
    "v_exp_f32 v39, v39\n"
    "s_waitcnt vmcnt(7)\n"
    "v_mfma_f32_32x32x4_2b_bf16 v[0:31], a[68:69], v[80:81], 0 cbsz:1 abid:0\n"
    "v_cvt_pk_bf16_f32 v34, v36, v37\n"
    "v_exp_f32 v40, v40\n"
    "v_exp_f32 v41, v41\n"
    "v_cvt_pk_bf16_f32 v35, v38, v39\n"
    "v_exp_f32 v42, v42\n"
    "v_exp_f32 v43, v43\n"
    "v_cvt_pk_bf16_f32 v40, v40, v41\n"
    "v_exp_f32 v44, v44\n"
    "v_exp_f32 v45, v45\n"
    "v_mfma_f32_32x32x16_bf16 a[32:47], a[112:115], v[32:35], a[32:47]\n"
    "v_cvt_pk_bf16_f32 v41, v42, v43\n"
    "v_exp_f32 v46, v46\n"
    "v_exp_f32 v47, v47\n"
    "v_cvt_pk_bf16_f32 v42, v44, v45\n"
    "v_exp_f32 v48, v48\n"
    "v_exp_f32 v49, v49\n"
    "v_cvt_pk_bf16_f32 v43, v46, v47\n"
    "v_exp_f32 v50, v50\n"
    "v_exp_f32 v51, v51\n"
    "v_mfma_f32_32x32x16_bf16 v[0:15], a[64:67], v[64:67], v[0:15]\n"
    "v_cvt_pk_bf16_f32 v48, v48, v49\n"
    "v_exp_f32 v52, v52\n"
    "v_exp_f32 v53, v53\n"
    "v_cvt_pk_bf16_f32 v49, v50, v51\n"
    "v_exp_f32 v54, v54\n"
    "v_exp_f32 v55, v55\n"
    "v_mfma_f32_32x32x16_bf16 a[32:47], a[116:119], v[40:43], a[32:47]\n"
    "v_cvt_pk_bf16_f32 v50, v52, v53\n"
    "v_exp_f32 v56, v56\n"
    "v_exp_f32 v57, v57\n"
    "v_cvt_pk_bf16_f32 v51, v54, v55\n"
    "v_exp_f32 v58, v58\n"
    "v_exp_f32 v59, v59\n"
    "v_mfma_f32_32x32x16_bf16 v[16:31], a[64:67], v[68:71], v[16:31]\n"
    "v_cvt_pk_bf16_f32 v56, v56, v57\n"
    "v_exp_f32 v60, v60\n"
    "v_exp_f32 v61, v61\n"
    "v_cvt_pk_bf16_f32 v57, v58, v59\n"
    "v_exp_f32 v62, v62\n"
    "v_exp_f32 v63, v63\n"
    "v_mfma_f32_32x32x16_bf16 a[48:63], a[112:115], v[48:51], a[48:63]\n"
    "s_sub_u32 s62, s62, 1\n"
    "s_cmp_lg_u32 s62, 0\n"
    "v_cvt_pk_bf16_f32 v58, v60, v61\n"
    "s_cbranch_scc1 ATT%=_loop\n"
    "v_exp_f32 v0, v0\n"
    "v_exp_f32 v1, v1\n"
    "v_cvt_pk_bf16_f32 v59, v62, v63\n"
    "v_exp_f32 v2, v2\n"
    "v_exp_f32 v3, v3\n"
    "v_mfma_f32_32x32x16_bf16 a[48:63], a[116:119], v[56:59], a[48:63]\n"
    "global_load_dwordx4 a[106:109], v84, s[60:61] sc1\n"
    "v_cvt_pk_bf16_f32 v0, v0, v1\n"
    "v_exp_f32 v4, v4\n"
    "v_exp_f32 v5, v5\n"
    "v_cvt_pk_bf16_f32 v1, v2, v3\n"
    "v_exp_f32 v6, v6\n"
    "v_exp_f32 v7, v7\n"
    "v_mfma_f32_32x32x4_2b_bf16 v[32:63], a[68:69], v[82:83], 0 cbsz:1 abid:0\n"
    "v_cvt_pk_bf16_f32 v2, v4, v5\n"
    "v_exp_f32 v8, v8\n"
    "v_exp_f32 v9, v9\n"
    "v_cvt_pk_bf16_f32 v3, v6, v7\n"
    "v_exp_f32 v10, v10\n"
    "v_exp_f32 v11, v11\n"
    "v_cvt_pk_bf16_f32 v8, v8, v9\n"
    "v_exp_f32 v12, v12\n"
    "v_exp_f32 v13, v13\n"
    "v_mfma_f32_32x32x16_bf16 a[0:15], a[70:73], v[0:3], a[0:15]\n"
    "global_load_dwordx4 a[112:115], v86, s[60:61] sc1\n"
    "v_cvt_pk_bf16_f32 v9, v10, v11\n"
    "v_exp_f32 v14, v14\n"
    "v_exp_f32 v15, v15\n"
    "v_cvt_pk_bf16_f32 v10, v12, v13\n"
    "v_exp_f32 v16, v16\n"
    "v_exp_f32 v17, v17\n"
    "v_cvt_pk_bf16_f32 v11, v14, v15\n"
    "v_exp_f32 v18, v18\n"
    "v_exp_f32 v19, v19\n"
    "v_mfma_f32_32x32x16_bf16 v[32:47], a[64:67], v[72:75], v[32:47]\n"
    "global_load_dwordx4 a[116:119], v86, s[60:61] offset:768 sc1\n"
    "v_cvt_pk_bf16_f32 v16, v16, v17\n"
    "v_exp_f32 v20, v20\n"
    "v_exp_f32 v21, v21\n"
    "v_cvt_pk_bf16_f32 v17, v18, v19\n"
    "v_exp_f32 v22, v22\n"
    "v_exp_f32 v23, v23\n"
    "v_mfma_f32_32x32x16_bf16 a[0:15], a[74:77], v[8:11], a[0:15]\n"
    "s_add_u32 s60, s60, 2816\n"
    "s_addc_u32 s61, s61, 0\n"
    "v_cvt_pk_bf16_f32 v18, v20, v21\n"
    "v_exp_f32 v24, v24\n"
    "v_exp_f32 v25, v25\n"
    "v_cvt_pk_bf16_f32 v19, v22, v23\n"
    "v_exp_f32 v26, v26\n"
    "v_exp_f32 v27, v27\n"
    "v_mfma_f32_32x32x16_bf16 v[48:63], a[64:67], v[76:79], v[48:63]\n"
    "v_cvt_pk_bf16_f32 v24, v24, v25\n"
    "v_exp_f32 v28, v28\n"
    "v_exp_f32 v29, v29\n"
    "v_cvt_pk_bf16_f32 v25, v26, v27\n"
    "v_exp_f32 v30, v30\n"
    "v_exp_f32 v31, v31\n"
    "v_mfma_f32_32x32x16_bf16 a[16:31], a[70:73], v[16:19], a[16:31]\n"
    "v_cvt_pk_bf16_f32 v26, v28, v29\n"
    "v_exp_f32 v32, v32\n"
    "v_exp_f32 v33, v33\n"
    "v_cvt_pk_bf16_f32 v27, v30, v31\n"
    "v_exp_f32 v34, v34\n"
    "v_exp_f32 v35, v35\n"
    "v_mfma_f32_32x32x16_bf16 a[16:31], a[74:77], v[24:27], a[16:31]\n"
    "v_cvt_pk_bf16_f32 v32, v32, v33\n"
    "v_exp_f32 v36, v36\n"
    "v_exp_f32 v37, v37\n"
    "v_cvt_pk_bf16_f32 v33, v34, v35\n"
    "v_exp_f32 v38, v38\n"
    "v_exp_f32 v39, v39\n"
    "s_waitcnt vmcnt(7)\n"
    "v_mfma_f32_32x32x4_2b_bf16 v[0:31], a[68:69], v[80:81], 0 cbsz:1 abid:1\n"
    "v_cvt_pk_bf16_f32 v34, v36, v37\n"
    "v_exp_f32 v40, v40\n"
    "v_exp_f32 v41, v41\n"
    "v_cvt_pk_bf16_f32 v35, v38, v39\n"
    "v_exp_f32 v42, v42\n"
    "v_exp_f32 v43, v43\n"
    "v_cvt_pk_bf16_f32 v40, v40, v41\n"
    "v_exp_f32 v44, v44\n"
    "v_exp_f32 v45, v45\n"
    "v_mfma_f32_32x32x16_bf16 a[32:47], a[70:73], v[32:35], a[32:47]\n"
    "v_cvt_pk_bf16_f32 v41, v42, v43\n"
    "v_exp_f32 v46, v46\n"
    "v_exp_f32 v47, v47\n"
    "v_cvt_pk_bf16_f32 v42, v44, v45\n"
    "v_exp_f32 v48, v48\n"
    "v_exp_f32 v49, v49\n"
    "v_cvt_pk_bf16_f32 v43, v46, v47\n"
    "v_exp_f32 v50, v50\n"
    "v_exp_f32 v51, v51\n"
    "v_mfma_f32_32x32x16_bf16 v[0:15], a[78:81], v[64:67], v[0:15]\n"
    "v_cvt_pk_bf16_f32 v48, v48, v49\n"
    "v_exp_f32 v52, v52\n"
    "v_exp_f32 v53, v53\n"
    "v_cvt_pk_bf16_f32 v49, v50, v51\n"
    "v_exp_f32 v54, v54\n"
    "v_exp_f32 v55, v55\n"
    "v_mfma_f32_32x32x16_bf16 a[32:47], a[74:77], v[40:43], a[32:47]\n"
    "v_cvt_pk_bf16_f32 v50, v52, v53\n"
    "v_exp_f32 v56, v56\n"
    "v_exp_f32 v57, v57\n"
    "v_cvt_pk_bf16_f32 v51, v54, v55\n"
    "v_exp_f32 v58, v58\n"
    "v_exp_f32 v59, v59\n"
    "v_mfma_f32_32x32x16_bf16 v[16:31], a[78:81], v[68:71], v[16:31]\n"
    "v_cvt_pk_bf16_f32 v56, v56, v57\n"
    "v_exp_f32 v60, v60\n"
    "v_exp_f32 v61, v61\n"
    "v_cvt_pk_bf16_f32 v57, v58, v59\n"
    "v_exp_f32 v62, v62\n"
    "v_exp_f32 v63, v63\n"
    "v_mfma_f32_32x32x16_bf16 a[48:63], a[70:73], v[48:51], a[48:63]\n"
    "v_cvt_pk_bf16_f32 v58, v60, v61\n"
    "v_exp_f32 v0, v0\n"
    "v_exp_f32 v1, v1\n"
    "v_cvt_pk_bf16_f32 v59, v62, v63\n"
    "v_exp_f32 v2, v2\n"
    "v_exp_f32 v3, v3\n"
    "v_mfma_f32_32x32x16_bf16 a[48:63], a[74:77], v[56:59], a[48:63]\n"
    "v_cvt_pk_bf16_f32 v0, v0, v1\n"
    "v_exp_f32 v4, v4\n"
    "v_exp_f32 v5, v5\n"
    "v_cvt_pk_bf16_f32 v1, v2, v3\n"
    "v_exp_f32 v6, v6\n"
    "v_exp_f32 v7, v7\n"
    "v_mfma_f32_32x32x4_2b_bf16 v[32:63], a[68:69], v[82:83], 0 cbsz:1 abid:1\n"
    "v_cvt_pk_bf16_f32 v2, v4, v5\n"
    "v_exp_f32 v8, v8\n"
    "v_exp_f32 v9, v9\n"
    "v_cvt_pk_bf16_f32 v3, v6, v7\n"
    "v_exp_f32 v10, v10\n"
    "v_exp_f32 v11, v11\n"
    "v_cvt_pk_bf16_f32 v8, v8, v9\n"
    "v_exp_f32 v12, v12\n"
    "v_exp_f32 v13, v13\n"
    "v_mfma_f32_32x32x16_bf16 a[0:15], a[84:87], v[0:3], a[0:15]\n"
    "v_cvt_pk_bf16_f32 v9, v10, v11\n"
    "v_exp_f32 v14, v14\n"
    "v_exp_f32 v15, v15\n"
    "v_cvt_pk_bf16_f32 v10, v12, v13\n"
    "v_exp_f32 v16, v16\n"
    "v_exp_f32 v17, v17\n"
    "v_cvt_pk_bf16_f32 v11, v14, v15\n"
    "v_exp_f32 v18, v18\n"
    "v_exp_f32 v19, v19\n"
    "v_mfma_f32_32x32x16_bf16 v[32:47], a[78:81], v[72:75], v[32:47]\n"
    "v_cvt_pk_bf16_f32 v16, v16, v17\n"
    "v_exp_f32 v20, v20\n"
    "v_exp_f32 v21, v21\n"
    "v_cvt_pk_bf16_f32 v17, v18, v19\n"
    "v_exp_f32 v22, v22\n"
    "v_exp_f32 v23, v23\n"
    "v_mfma_f32_32x32x16_bf16 a[0:15], a[88:91], v[8:11], a[0:15]\n"
    "v_cvt_pk_bf16_f32 v18, v20, v21\n"
    "v_exp_f32 v24, v24\n"
    "v_exp_f32 v25, v25\n"
    "v_cvt_pk_bf16_f32 v19, v22, v23\n"
    "v_exp_f32 v26, v26\n"
    "v_exp_f32 v27, v27\n"
    "v_mfma_f32_32x32x16_bf16 v[48:63], a[78:81], v[76:79], v[48:63]\n"
    "v_cvt_pk_bf16_f32 v24, v24, v25\n"
    "v_exp_f32 v28, v28\n"
    "v_exp_f32 v29, v29\n"
    "v_cvt_pk_bf16_f32 v25, v26, v27\n"
    "v_exp_f32 v30, v30\n"
    "v_exp_f32 v31, v31\n"
    "v_mfma_f32_32x32x16_bf16 a[16:31], a[84:87], v[16:19], a[16:31]\n"
    "v_cvt_pk_bf16_f32 v26, v28, v29\n"
    "v_exp_f32 v32, v32\n"
    "v_exp_f32 v33, v33\n"
    "v_cvt_pk_bf16_f32 v27, v30, v31\n"
    "v_exp_f32 v34, v34\n"
    "v_exp_f32 v35, v35\n"
    "v_mfma_f32_32x32x16_bf16 a[16:31], a[88:91], v[24:27], a[16:31]\n"
    "v_cvt_pk_bf16_f32 v32, v32, v33\n"
    "v_exp_f32 v36, v36\n"
    "v_exp_f32 v37, v37\n"
    "v_cvt_pk_bf16_f32 v33, v34, v35\n"
    "v_exp_f32 v38, v38\n"
    "v_exp_f32 v39, v39\n"
    "s_waitcnt vmcnt(3)\n"
    "v_mfma_f32_32x32x4_2b_bf16 v[0:31], a[96:97], v[80:81], 0 cbsz:1 abid:0\n"
    "v_cvt_pk_bf16_f32 v34, v36, v37\n"
    "v_exp_f32 v40, v40\n"
    "v_exp_f32 v41, v41\n"
    "v_cvt_pk_bf16_f32 v35, v38, v39\n"
    "v_exp_f32 v42, v42\n"
    "v_exp_f32 v43, v43\n"
    "v_cvt_pk_bf16_f32 v40, v40, v41\n"
    "v_exp_f32 v44, v44\n"
    "v_exp_f32 v45, v45\n"
    "v_mfma_f32_32x32x16_bf16 a[32:47], a[84:87], v[32:35], a[32:47]\n"
    "v_cvt_pk_bf16_f32 v41, v42, v43\n"
    "v_exp_f32 v46, v46\n"
    "v_exp_f32 v47, v47\n"
    "v_cvt_pk_bf16_f32 v42, v44, v45\n"
    "v_exp_f32 v48, v48\n"
    "v_exp_f32 v49, v49\n"
    "v_cvt_pk_bf16_f32 v43, v46, v47\n"
    "v_exp_f32 v50, v50\n"
    "v_exp_f32 v51, v51\n"
    "v_mfma_f32_32x32x16_bf16 v[0:15], a[92:95], v[64:67], v[0:15]\n"
    "v_cvt_pk_bf16_f32 v48, v48, v49\n"
    "v_exp_f32 v52, v52\n"
    "v_exp_f32 v53, v53\n"
    "v_cvt_pk_bf16_f32 v49, v50, v51\n"
    "v_exp_f32 v54, v54\n"
    "v_exp_f32 v55, v55\n"
    "v_mfma_f32_32x32x16_bf16 a[32:47], a[88:91], v[40:43], a[32:47]\n"
    "v_cvt_pk_bf16_f32 v50, v52, v53\n"
    "v_exp_f32 v56, v56\n"
    "v_exp_f32 v57, v57\n"
    "v_cvt_pk_bf16_f32 v51, v54, v55\n"
    "v_exp_f32 v58, v58\n"
    "v_exp_f32 v59, v59\n"
    "v_mfma_f32_32x32x16_bf16 v[16:31], a[92:95], v[68:71], v[16:31]\n"
    "v_cvt_pk_bf16_f32 v56, v56, v57\n"
    "v_exp_f32 v60, v60\n"
    "v_exp_f32 v61, v61\n"
    "v_cvt_pk_bf16_f32 v57, v58, v59\n"
    "v_exp_f32 v62, v62\n"
    "v_exp_f32 v63, v63\n"
    "v_mfma_f32_32x32x16_bf16 a[48:63], a[84:87], v[48:51], a[48:63]\n"
    "v_cvt_pk_bf16_f32 v58, v60, v61\n"
    "v_exp_f32 v0, v0\n"
    "v_exp_f32 v1, v1\n"
    "v_cvt_pk_bf16_f32 v59, v62, v63\n"
    "v_exp_f32 v2, v2\n"
    "v_exp_f32 v3, v3\n"
    "v_mfma_f32_32x32x16_bf16 a[48:63], a[88:91], v[56:59], a[48:63]\n"
    "v_cvt_pk_bf16_f32 v0, v0, v1\n"
    "v_exp_f32 v4, v4\n"
    "v_exp_f32 v5, v5\n"
    "v_cvt_pk_bf16_f32 v1, v2, v3\n"
    "v_exp_f32 v6, v6\n"
    "v_exp_f32 v7, v7\n"
    "v_mfma_f32_32x32x4_2b_bf16 v[32:63], a[96:97], v[82:83], 0 cbsz:1 abid:0\n"
    "v_cvt_pk_bf16_f32 v2, v4, v5\n"
    "v_exp_f32 v8, v8\n"
    "v_exp_f32 v9, v9\n"
    "v_cvt_pk_bf16_f32 v3, v6, v7\n"
    "v_exp_f32 v10, v10\n"
    "v_exp_f32 v11, v11\n"
    "v_cvt_pk_bf16_f32 v8, v8, v9\n"
    "v_exp_f32 v12, v12\n"
    "v_exp_f32 v13, v13\n"
    "v_mfma_f32_32x32x16_bf16 a[0:15], a[98:101], v[0:3], a[0:15]\n"
    "v_cvt_pk_bf16_f32 v9, v10, v11\n"
    "v_exp_f32 v14, v14\n"
    "v_exp_f32 v15, v15\n"
    "v_cvt_pk_bf16_f32 v10, v12, v13\n"
    "v_exp_f32 v16, v16\n"
    "v_exp_f32 v17, v17\n"
    "v_cvt_pk_bf16_f32 v11, v14, v15\n"
    "v_exp_f32 v18, v18\n"
    "v_exp_f32 v19, v19\n"
    "v_mfma_f32_32x32x16_bf16 v[32:47], a[92:95], v[72:75], v[32:47]\n"
    "v_cvt_pk_bf16_f32 v16, v16, v17\n"
    "v_exp_f32 v20, v20\n"
    "v_exp_f32 v21, v21\n"
    "v_cvt_pk_bf16_f32 v17, v18, v19\n"
    "v_exp_f32 v22, v22\n"
    "v_exp_f32 v23, v23\n"
    "v_mfma_f32_32x32x16_bf16 a[0:15], a[102:105], v[8:11], a[0:15]\n"
    "v_cvt_pk_bf16_f32 v18, v20, v21\n"
    "v_exp_f32 v24, v24\n"
    "v_exp_f32 v25, v25\n"
    "v_cvt_pk_bf16_f32 v19, v22, v23\n"
    "v_exp_f32 v26, v26\n"
    "v_exp_f32 v27, v27\n"
    "v_mfma_f32_32x32x16_bf16 v[48:63], a[92:95], v[76:79], v[48:63]\n"
    "v_cvt_pk_bf16_f32 v24, v24, v25\n"
    "v_exp_f32 v28, v28\n"
    "v_exp_f32 v29, v29\n"
    "v_cvt_pk_bf16_f32 v25, v26, v27\n"
    "v_exp_f32 v30, v30\n"
    "v_exp_f32 v31, v31\n"
    "v_mfma_f32_32x32x16_bf16 a[16:31], a[98:101], v[16:19], a[16:31]\n"
    "v_cvt_pk_bf16_f32 v26, v28, v29\n"
    "v_exp_f32 v32, v32\n"
    "v_exp_f32 v33, v33\n"
    "v_cvt_pk_bf16_f32 v27, v30, v31\n"
    "v_exp_f32 v34, v34\n"
    "v_exp_f32 v35, v35\n"
    "v_mfma_f32_32x32x16_bf16 a[16:31], a[102:105], v[24:27], a[16:31]\n"
    "v_cvt_pk_bf16_f32 v32, v32, v33\n"
    "v_exp_f32 v36, v36\n"
    "v_exp_f32 v37, v37\n"
    "v_cvt_pk_bf16_f32 v33, v34, v35\n"
    "v_exp_f32 v38, v38\n"
    "v_exp_f32 v39, v39\n"
    "s_waitcnt vmcnt(0)\n"
    "v_mfma_f32_32x32x4_2b_bf16 v[0:31], a[96:97], v[82:83], 0 cbsz:1 abid:1\n"
    "v_cvt_pk_bf16_f32 v34, v36, v37\n"
    "v_exp_f32 v40, v40\n"
    "v_exp_f32 v41, v41\n"
    "v_cvt_pk_bf16_f32 v35, v38, v39\n"
    "v_exp_f32 v42, v42\n"
    "v_exp_f32 v43, v43\n"
    "v_cvt_pk_bf16_f32 v40, v40, v41\n"
    "v_exp_f32 v44, v44\n"
    "v_exp_f32 v45, v45\n"
    "v_mfma_f32_32x32x16_bf16 a[32:47], a[98:101], v[32:35], a[32:47]\n"
    "v_cvt_pk_bf16_f32 v41, v42, v43\n"
    "v_exp_f32 v46, v46\n"
    "v_exp_f32 v47, v47\n"
    "v_cvt_pk_bf16_f32 v42, v44, v45\n"
    "v_exp_f32 v48, v48\n"
    "v_exp_f32 v49, v49\n"
    "v_cvt_pk_bf16_f32 v43, v46, v47\n"
    "v_exp_f32 v50, v50\n"
    "v_exp_f32 v51, v51\n"
    "v_mfma_f32_32x32x16_bf16 v[0:15], a[106:109], v[72:75], v[0:15]\n"
    "v_cvt_pk_bf16_f32 v48, v48, v49\n"
    "v_exp_f32 v52, v52\n"
    "v_exp_f32 v53, v53\n"
    "v_cvt_pk_bf16_f32 v49, v50, v51\n"
    "v_exp_f32 v54, v54\n"
    "v_exp_f32 v55, v55\n"
    "v_mfma_f32_32x32x16_bf16 a[32:47], a[102:105], v[40:43], a[32:47]\n"
    "v_cvt_pk_bf16_f32 v50, v52, v53\n"
    "v_exp_f32 v56, v56\n"
    "v_exp_f32 v57, v57\n"
    "v_cvt_pk_bf16_f32 v51, v54, v55\n"
    "v_exp_f32 v58, v58\n"
    "v_exp_f32 v59, v59\n"
    "v_mfma_f32_32x32x16_bf16 v[16:31], a[106:109], v[76:79], v[16:31]\n"
    "v_cvt_pk_bf16_f32 v56, v56, v57\n"
    "v_exp_f32 v60, v60\n"
    "v_exp_f32 v61, v61\n"
    "v_cvt_pk_bf16_f32 v57, v58, v59\n"
    "v_exp_f32 v62, v62\n"
    "v_exp_f32 v63, v63\n"
    "v_mfma_f32_32x32x16_bf16 a[48:63], a[98:101], v[48:51], a[48:63]\n"
    "v_cvt_pk_bf16_f32 v58, v60, v61\n"
    "v_exp_f32 v0, v0\n"
    "v_exp_f32 v1, v1\n"
    "v_cvt_pk_bf16_f32 v59, v62, v63\n"
    "v_exp_f32 v2, v2\n"
    "v_exp_f32 v3, v3\n"
    "v_mfma_f32_32x32x16_bf16 a[48:63], a[102:105], v[56:59], a[48:63]\n"
    "v_cvt_pk_bf16_f32 v0, v0, v1\n"
    "v_exp_f32 v4, v4\n"
    "v_exp_f32 v5, v5\n"
    "v_cvt_pk_bf16_f32 v1, v2, v3\n"
    "v_exp_f32 v6, v6\n"
    "v_exp_f32 v7, v7\n"
    "v_mfma_f32_32x32x4_2b_bf16 v[32:63], a[96:97], v[80:81], 0 cbsz:1 abid:1\n"
    "v_cvt_pk_bf16_f32 v2, v4, v5\n"
    "v_exp_f32 v8, v8\n"
    "v_exp_f32 v9, v9\n"
    "v_cvt_pk_bf16_f32 v3, v6, v7\n"
    "v_exp_f32 v10, v10\n"
    "v_exp_f32 v11, v11\n"
    "v_cvt_pk_bf16_f32 v8, v8, v9\n"
    "v_exp_f32 v12, v12\n"
    "v_exp_f32 v13, v13\n"
    "v_mfma_f32_32x32x16_bf16 a[32:47], a[112:115], v[0:3], a[32:47]\n"
    "v_cvt_pk_bf16_f32 v9, v10, v11\n"
    "v_exp_f32 v14, v14\n"
    "v_exp_f32 v15, v15\n"
    "v_cvt_pk_bf16_f32 v10, v12, v13\n"
    "v_exp_f32 v16, v16\n"
    "v_exp_f32 v17, v17\n"
    "v_cvt_pk_bf16_f32 v11, v14, v15\n"
    "v_exp_f32 v18, v18\n"
    "v_exp_f32 v19, v19\n"
    "v_mfma_f32_32x32x16_bf16 v[32:47], a[106:109], v[64:67], v[32:47]\n"
    "v_cvt_pk_bf16_f32 v16, v16, v17\n"
    "v_exp_f32 v20, v20\n"
    "v_exp_f32 v21, v21\n"
    "v_cvt_pk_bf16_f32 v17, v18, v19\n"
    "v_exp_f32 v22, v22\n"
    "v_exp_f32 v23, v23\n"
    "v_mfma_f32_32x32x16_bf16 a[32:47], a[116:119], v[8:11], a[32:47]\n"
    "v_cvt_pk_bf16_f32 v18, v20, v21\n"
    "v_exp_f32 v24, v24\n"
    "v_exp_f32 v25, v25\n"
    "v_cvt_pk_bf16_f32 v19, v22, v23\n"
    "v_exp_f32 v26, v26\n"
    "v_exp_f32 v27, v27\n"
    "v_mfma_f32_32x32x16_bf16 v[48:63], a[106:109], v[68:71], v[48:63]\n"
    "v_cvt_pk_bf16_f32 v24, v24, v25\n"
    "v_exp_f32 v28, v28\n"
    "v_exp_f32 v29, v29\n"
    "v_cvt_pk_bf16_f32 v25, v26, v27\n"
    "v_exp_f32 v30, v30\n"
    "v_exp_f32 v31, v31\n"
    "v_mfma_f32_32x32x16_bf16 a[48:63], a[112:115], v[16:19], a[48:63]\n"
    "v_cvt_pk_bf16_f32 v26, v28, v29\n"
    "v_exp_f32 v32, v32\n"
    "v_exp_f32 v33, v33\n"
    "v_cvt_pk_bf16_f32 v27, v30, v31\n"
    "v_exp_f32 v34, v34\n"
    "v_exp_f32 v35, v35\n"
    "v_mfma_f32_32x32x16_bf16 a[48:63], a[116:119], v[24:27], a[48:63]\n"
    "v_cvt_pk_bf16_f32 v32, v32, v33\n"
    "v_exp_f32 v36, v36\n"
    "v_exp_f32 v37, v37\n"
    "v_cvt_pk_bf16_f32 v33, v34, v35\n"
    "v_exp_f32 v38, v38\n"
    "v_exp_f32 v39, v39\n"
    "v_cvt_pk_bf16_f32 v34, v36, v37\n"
    "v_exp_f32 v40, v40\n"
    "v_exp_f32 v41, v41\n"
    "v_cvt_pk_bf16_f32 v35, v38, v39\n"
    "v_exp_f32 v42, v42\n"
    "v_exp_f32 v43, v43\n"
    "v_cvt_pk_bf16_f32 v40, v40, v41\n"
    "v_exp_f32 v44, v44\n"
    "v_exp_f32 v45, v45\n"
    "v_mfma_f32_32x32x16_bf16 a[0:15], a[112:115], v[32:35], a[0:15]\n"
    "v_cvt_pk_bf16_f32 v41, v42, v43\n"
    "v_exp_f32 v46, v46\n"
    "v_exp_f32 v47, v47\n"
    "v_cvt_pk_bf16_f32 v42, v44, v45\n"
    "ds_write_b128 v87, a[32:35] offset:0\n"
    "v_exp_f32 v48, v48\n"
    "v_exp_f32 v49, v49\n"
    "v_cvt_pk_bf16_f32 v43, v46, v47\n"
    "ds_write_b128 v87, a[36:39] offset:1024\n"
    "v_exp_f32 v50, v50\n"
    "v_exp_f32 v51, v51\n"
    "v_cvt_pk_bf16_f32 v48, v48, v49\n"
    "ds_write_b128 v87, a[40:43] offset:2048\n"
    "v_exp_f32 v52, v52\n"
    "v_exp_f32 v53, v53\n"
    "v_cvt_pk_bf16_f32 v49, v50, v51\n"
    "ds_write_b128 v87, a[48:51] offset:3072\n"
    "v_exp_f32 v54, v54\n"
    "v_exp_f32 v55, v55\n"
    "v_mfma_f32_32x32x16_bf16 a[0:15], a[116:119], v[40:43], a[0:15]\n"
    "v_cvt_pk_bf16_f32 v50, v52, v53\n"
    "ds_write_b128 v87, a[52:55] offset:4096\n"
    "v_exp_f32 v56, v56\n"
    "v_exp_f32 v57, v57\n"
    "v_cvt_pk_bf16_f32 v51, v54, v55\n"
    "ds_write_b128 v87, a[56:59] offset:5120\n"
    "v_exp_f32 v58, v58\n"
    "v_exp_f32 v59, v59\n"
    "v_cvt_pk_bf16_f32 v56, v56, v57\n"
    "v_exp_f32 v60, v60\n"
    "v_exp_f32 v61, v61\n"
    "v_cvt_pk_bf16_f32 v57, v58, v59\n"
    "s_waitcnt lgkmcnt(0)\n"
    "s_mov_b64 exec, 1\n"
    "ds_write_b32 v95, v88 offset:16\n"
    "s_mov_b64 exec, -1\n"
    "ds_read_b32 v90, v92\n"
    "ds_read_b32 v91, v92 offset:128\n"
    "v_exp_f32 v62, v62\n"
    "v_exp_f32 v63, v63\n"
    "v_mfma_f32_32x32x16_bf16 a[16:31], a[112:115], v[48:51], a[16:31]\n"
    "v_cvt_pk_bf16_f32 v58, v60, v61\n"
    "s_nop 0\n"
    "v_cvt_pk_bf16_f32 v59, v62, v63\n"
    "s_nop 1\n"
    "v_mfma_f32_32x32x16_bf16 a[16:31], a[116:119], v[56:59], a[16:31]\n"
    "s_mov_b32 s62, 0x100000\n"
    "OXS%=:\n"
    "ds_read_b32 v48, v96 offset:16\n"
    "ds_read_b32 v49, v89 offset:4\n"
    "s_sub_u32 s62, s62, 1\n"
    "s_cmp_eq_u32 s62, 0\n"
    "s_cbranch_scc1 OXX%=\n"
    "s_waitcnt lgkmcnt(0)\n"
    "v_mul_u32_u24 v48, v48, v49\n"
    "s_nop 0\n"
    "v_readfirstlane_b32 s63, v48\n"
    "s_nop 0\n"
    "s_cmp_lt_u32 s63, 9\n"
    "s_cbranch_scc1 OXS%=\n"
    "OXX%=:\n"
    "s_waitcnt lgkmcnt(0)\n"
    "ds_read_b128 v[24:27], v94 offset:0\n"
    "ds_read_b128 v[28:31], v94 offset:1024\n"
    "ds_read_b128 v[32:35], v94 offset:2048\n"
    "ds_read_b128 v[36:39], v94 offset:3072\n"
    "ds_read_b128 v[40:43], v94 offset:4096\n"
    "ds_read_b128 v[44:47], v94 offset:5120\n"
    "v_accvgpr_read_b32 v0, a0\n"
    "v_accvgpr_read_b32 v1, a1\n"
    "v_accvgpr_read_b32 v2, a2\n"
    "v_accvgpr_read_b32 v3, a3\n"
    "v_accvgpr_read_b32 v4, a4\n"
    "v_accvgpr_read_b32 v5, a5\n"
    "v_accvgpr_read_b32 v6, a6\n"
    "v_accvgpr_read_b32 v7, a7\n"
    "v_accvgpr_read_b32 v8, a8\n"
    "v_accvgpr_read_b32 v9, a9\n"
    "v_accvgpr_read_b32 v10, a10\n"
    "v_accvgpr_read_b32 v11, a11\n"
    "v_accvgpr_read_b32 v12, a16\n"
    "v_accvgpr_read_b32 v13, a17\n"
    "v_accvgpr_read_b32 v14, a18\n"
    "v_accvgpr_read_b32 v15, a19\n"
    "v_accvgpr_read_b32 v16, a20\n"
    "v_accvgpr_read_b32 v17, a21\n"
    "v_accvgpr_read_b32 v18, a22\n"
    "v_accvgpr_read_b32 v19, a23\n"
    "v_accvgpr_read_b32 v20, a24\n"
    "v_accvgpr_read_b32 v21, a25\n"
    "v_accvgpr_read_b32 v22, a26\n"
    "v_accvgpr_read_b32 v23, a27\n"
    "s_waitcnt lgkmcnt(5)\n"
    "v_pk_add_f32 v[0:1], v[0:1], v[24:25]\n"
    "v_pk_add_f32 v[2:3], v[2:3], v[26:27]\n"
    "s_waitcnt lgkmcnt(4)\n"
    "v_pk_add_f32 v[4:5], v[4:5], v[28:29]\n"
    "v_pk_add_f32 v[6:7], v[6:7], v[30:31]\n"
    "s_waitcnt lgkmcnt(3)\n"
    "v_pk_add_f32 v[8:9], v[8:9], v[32:33]\n"
    "v_pk_add_f32 v[10:11], v[10:11], v[34:35]\n"
    "s_waitcnt lgkmcnt(2)\n"
    "v_pk_add_f32 v[12:13], v[12:13], v[36:37]\n"
    "v_pk_add_f32 v[14:15], v[14:15], v[38:39]\n"
    "s_waitcnt lgkmcnt(1)\n"
    "v_pk_add_f32 v[16:17], v[16:17], v[40:41]\n"
    "v_pk_add_f32 v[18:19], v[18:19], v[42:43]\n"
    "s_waitcnt lgkmcnt(0)\n"
    "v_pk_add_f32 v[20:21], v[20:21], v[44:45]\n"
    "v_pk_add_f32 v[22:23], v[22:23], v[46:47]\n"
    "v_mov_b32 v48, v8\n"
    "v_mov_b32 v49, v8\n"
    "v_mov_b32 v50, v20\n"
    "v_mov_b32 v51, v20\n"
    "v_pk_add_f32 v[24:25], v[0:1], v[2:3]\n"
    "v_pk_add_f32 v[26:27], v[4:5], v[6:7]\n"
    "v_permlane32_swap_b32 v48, v49\n"
    "v_permlane32_swap_b32 v50, v51\n"
    "v_pk_add_f32 v[24:25], v[24:25], v[8:9]\n"
    "v_pk_add_f32 v[26:27], v[26:27], v[10:11]\n"
    "v_pk_add_f32 v[24:25], v[24:25], v[12:13]\n"
    "v_pk_add_f32 v[26:27], v[26:27], v[14:15]\n"
    "v_pk_add_f32 v[24:25], v[24:25], v[16:17]\n"
    "v_pk_add_f32 v[26:27], v[26:27], v[18:19]\n"
    "v_pk_add_f32 v[24:25], v[24:25], v[20:21]\n"
    "v_pk_add_f32 v[26:27], v[26:27], v[22:23]\n"
    "v_mul_f32 v90, v90, v49\n"
    "v_mul_f32 v91, v91, v51\n"
    "v_pk_add_f32 v[24:25], v[24:25], v[26:27]\n"
    "v_rcp_f32 v40, v90\n"
    "v_rcp_f32 v42, v91\n"
    "v_add_f32 v24, v24, v25\n"
    "v_mov_b32 v41, v40\n"
    "v_mov_b32 v43, v42\n"
    "v_mov_b32 v25, 0x1f8\n"
    "v_min_f32 v26, v49, v51\n"
    "v_max3_f32 v27, v49, v51, v90\n"
    "v_min_f32 v28, v90, v91\n"
    "v_max_f32 v27, v27, v91\n"
    "v_cmp_class_f32 s[62:63], v24, v25\n"
    "v_cmp_lt_f32 vcc, 0x12800000, v26\n"
    "s_nop 1\n"
    "s_and_b64 s[62:63], s[62:63], vcc\n"
    "v_cmp_gt_f32 vcc, 0x7a800000, v27\n"
    "s_nop 1\n"
    "s_and_b64 s[62:63], s[62:63], vcc\n"
    "v_cmp_lt_f32 vcc, 0x03800000, v28\n"
    "s_nop 1\n"
    "s_and_b64 s[62:63], s[62:63], vcc\n"
    "s_mov_b32 %[bad], 1\n"
    "s_cmp_lg_u64 s[62:63], exec\n"
    "s_cbranch_scc1 OE%=\n"
    "s_mov_b32 %[bad], 0\n"
    "v_pk_mul_f32 v[0:1], v[0:1], v[40:41]\n"
    "v_pk_mul_f32 v[12:13], v[12:13], v[42:43]\n"
    "v_pk_mul_f32 v[2:3], v[2:3], v[40:41]\n"
    "v_pk_mul_f32 v[14:15], v[14:15], v[42:43]\n"
    "v_pk_mul_f32 v[4:5], v[4:5], v[40:41]\n"
    "v_pk_mul_f32 v[16:17], v[16:17], v[42:43]\n"
    "v_pk_mul_f32 v[6:7], v[6:7], v[40:41]\n"
    "v_pk_mul_f32 v[18:19], v[18:19], v[42:43]\n"
    "v_pk_mul_f32 v[8:9], v[8:9], v[40:41]\n"
    "v_pk_mul_f32 v[20:21], v[20:21], v[42:43]\n"
    "v_pk_mul_f32 v[10:11], v[10:11], v[40:41]\n"
    "v_pk_mul_f32 v[22:23], v[22:23], v[42:43]\n"
    "v_cvt_pk_bf16_f32 v24, v0, v1\n"
    "v_cvt_pk_bf16_f32 v25, v2, v3\n"
    "v_cvt_pk_bf16_f32 v26, v4, v5\n"
    "v_cvt_pk_bf16_f32 v27, v6, v7\n"
    "global_store_dwordx4 v97, v[24:27], %[ob] sc1\n"
    "ds_write_b128 v97, v[24:27] offset:20480\n"
    "v_cvt_pk_bf16_f32 v28, v12, v13\n"
    "v_cvt_pk_bf16_f32 v29, v14, v15\n"
    "v_cvt_pk_bf16_f32 v30, v16, v17\n"
    "v_cvt_pk_bf16_f32 v31, v18, v19\n"
    "global_store_dwordx4 v97, v[28:31], %[ob] offset:1024 sc1\n"
    "ds_write_b128 v97, v[28:31] offset:21504\n"
    "v_cvt_pk_bf16_f32 v32, v8, v9\n"
    "v_cvt_pk_bf16_f32 v33, v10, v11\n"
    "v_cvt_pk_bf16_f32 v34, v20, v21\n"
    "v_cvt_pk_bf16_f32 v35, v22, v23\n"
    "v_lshlrev_b32 v36, 9, v84\n"
    "v_mov_b64 v[38:39], %[ep]\n"
    "v_permlane32_swap_b32 v32, v34\n"
    "v_permlane32_swap_b32 v33, v35\n"
    "s_nop 0\n"
    "global_store_dwordx2 v98, v[32:33], %[ob] sc1\n"
    "ds_write_b64 v98, v[32:33] offset:20480\n"
    "s_waitcnt lgkmcnt(0)\n"
    "s_mov_b64 exec, 3\n"
    "global_store_dwordx2 v36, v[38:39], %[ofl] sc1\n"
    "s_mov_b64 exec, 1\n"
    "ds_add_u32 v89, v88 offset:8\n"
    "s_mov_b64 exec, -1\n"
    "OE%=:\n"
      : "=&{v[64:67]}"(q0a), "=&{v[68:71]}"(q0b), "=&{v[80:81]}"(q1), "+{v54}"(junk0), "=&{v[100:107]}"(xres0), "=&{v[108:109]}"(xres1), [bad] "=&s"(bad)
      : "{v[84:87]}"(a84), "{v[92:99]}"(a92), "{v[110:113]}"(a110), "{v[114:121]}"(xbv), "{v[122:123]}"(a122),
        [srd] "s"(srdq), [soff] "s"(qsrc & ~15u), [m0b] "s"(L0 + L_AR + 6144u * q), [m0c] "s"(L0 + q * PS + 20480u), [flb] "s"(L0 + L_FL),
        [kb] "s"(kb), [xp] "s"(x), [ob] "s"(ob), [ofl] "s"(ofl), [ep] "s"(epoch)
      : "v0", "v1", "v2", "v3", "v4", "v5", "v6", "v7", "v8", "v9", "v10", "v11", "v12", "v13", "v14", "v15", "v16", "v17", "v18", "v19", "v20", "v21", "v22", "v23", "v24", "v25", "v26", "v27", "v28", "v29", "v30", "v31", "v32", "v33", "v34", "v35", "v36", "v37", "v38", "v39", "v40", "v41", "v42", "v43", "v44", "v45", "v46", "v47", "v48", "v49", "v50", "v51", "v52", "v53", "v55", "v56", "v57", "v58", "v59", "v60", "v61", "v62", "v63", "v72", "v73", "v74", "v75", "v76", "v77", "v78", "v79", "v82", "v83", "v88", "v89", "v90", "v91", "a0", "a1", "a2", "a3", "a4", "a5", "a6", "a7", "a8", "a9", "a10", "a11", "a12", "a13", "a14", "a15", "a16", "a17", "a18", "a19", "a20", "a21", "a22", "a23", "a24", "a25", "a26", "a27", "a28", "a29", "a30", "a31", "a32", "a33", "a34", "a35", "a36", "a37", "a38", "a39", "a40", "a41", "a42", "a43", "a44", "a45", "a46", "a47", "a48", "a49", "a50", "a51", "a52", "a53", "a54", "a55", "a56", "a57", "a58", "a59", "a60", "a61", "a62", "a63", "a64", "a65", "a66", "a67", "a68", "a69", "a70", "a71", "a72", "a73", "a74", "a75", "a76", "a77", "a78", "a79", "a80", "a81", "a82", "a83", "a84", "a85", "a86", "a87", "a88", "a89", "a90", "a91", "a92", "a93", "a94", "a95", "a96", "a97", "a98", "a99", "a100", "a101", "a102", "a103", "a104", "a105", "a106", "a107", "a108", "a109", "a110", "a111", "a112", "a113", "a114", "a115", "a116", "a117", "a118", "a119", "s58", "s59", "s60", "s61", "s62", "s63", "s64", "s65", "vcc", "scc", "memory");
    LANE_VARS
    const int rr = 8 * q + (lane >> 3), cg = lane & 7;
    if (__builtin_expect(bad != 0, 0)) {
#pragma unroll 1
      for (int u = 0; u < 2; ++u) {
        u32x2 qx; u32x4 fr; u32x2 lf;
#pragma unroll
        for (int i = 0; i < 2; ++i) { const u32 a = q1[i], up = f2u(hi_half(u2f(a))); qx[i] = hi ? 0u : (u ? up : a); }
        attn_exact(ws + W_KV, a84[0], 1024u + r * 8, a84[2], u ? q0b : q0a, qx, *(LAS float*)(lds + L_RS + u * 128 + r * 4), fr, lf);
        st16(ob, a92[5] + u * 1024, fr); *(LAS u32x4*)(lds + 20480 + a92[5] + u * 1024) = fr;
        if (!hi) { st8(ob + a92[6] + u * 1024, ((u64)lf[1] << 32) | lf[0]); *(LAS u32x2*)(lds + 20480 + a92[6] + u * 1024) = lf; }
      }
      drain(); ldrain();
      if (lane < 2) st8(ofl + lane * 8192, epoch);
      if (lane == 0) lds_add(lds + L_FL + 8);
    }
    asm volatile(
    "ds_read_b32 v64, %[ra]\n"
    "ds_read_b32 v65, %[ra]\n"
    "ds_read_b32 v66, %[rb]\n"
    "ds_read_b32 v67, %[rb]\n"
    "v_lshlrev_b32 v44, 16, v100\n"
    "v_and_b32 v45, 0xffff0000, v100\n"
    "v_lshlrev_b32 v46, 16, v101\n"
    "v_and_b32 v47, 0xffff0000, v101\n"
    "v_lshlrev_b32 v48, 16, v102\n"
    "v_and_b32 v49, 0xffff0000, v102\n"
    "v_lshlrev_b32 v50, 16, v103\n"
    "v_and_b32 v51, 0xffff0000, v103\n"
    "v_lshlrev_b32 v52, 16, v104\n"
    "v_and_b32 v53, 0xffff0000, v104\n"
    "v_lshlrev_b32 v54, 16, v105\n"
    "v_and_b32 v55, 0xffff0000, v105\n"
    "v_lshlrev_b32 v56, 16, v106\n"
    "v_and_b32 v57, 0xffff0000, v106\n"
    "v_lshlrev_b32 v58, 16, v107\n"
    "v_and_b32 v59, 0xffff0000, v107\n"
    "v_lshlrev_b32 v60, 16, v108\n"
    "v_and_b32 v61, 0xffff0000, v108\n"
    "v_lshlrev_b32 v62, 16, v109\n"
    "v_and_b32 v63, 0xffff0000, v109\n"
    "v_add_u32 v42, %[la], %[ps2]\n"
    "v_mov_b32 v68, %[flb]\n"
    "s_setprio 0\n"
    "s_waitcnt lgkmcnt(0)\n"
    "s_barrier\n"
    "ds_read_b128 v[0:3], %[la] offset:0\n"
    "ds_read_b128 v[4:7], %[la] offset:34752\n"
    "ds_read_b128 v[8:11], v42 offset:0\n"
    "ds_read_b128 v[12:15], v42 offset:34752\n"
    "ds_read_b128 v[16:19], %[la] offset:4096\n"
    "ds_read_b128 v[20:23], %[la] offset:38848\n"
    "ds_read_b128 v[24:27], v42 offset:4096\n"
    "ds_read_b128 v[28:31], v42 offset:38848\n"
    "s_waitcnt lgkmcnt(4)\n"
    "v_pk_add_f32 v[0:1], v[0:1], v[4:5]\n"
    "v_pk_add_f32 v[2:3], v[2:3], v[6:7]\n"
    "v_pk_add_f32 v[8:9], v[8:9], v[12:13]\n"
    "v_pk_add_f32 v[10:11], v[10:11], v[14:15]\n"
    "v_pk_add_f32 v[32:33], v[0:1], v[8:9]\n"
    "v_pk_add_f32 v[34:35], v[2:3], v[10:11]\n"
    "ds_read_b128 v[0:3], %[la] offset:8192\n"
    "ds_read_b128 v[4:7], %[la] offset:42944\n"
    "ds_read_b128 v[8:11], v42 offset:8192\n"
    "ds_read_b128 v[12:15], v42 offset:42944\n"
    "v_pk_fma_f32 v[32:33], v[32:33], v[64:65], v[44:45]\n"
    "v_pk_fma_f32 v[34:35], v[34:35], v[64:65], v[46:47]\n"
    "v_cvt_pk_bf16_f32 v36, v32, v33\n"
    "v_cvt_pk_bf16_f32 v37, v34, v35\n"
    "global_store_dwordx2 v110, v[36:37], %[outp] offset:0\n"
    "s_waitcnt lgkmcnt(4)\n"
    "v_pk_add_f32 v[16:17], v[16:17], v[20:21]\n"
    "v_pk_add_f32 v[18:19], v[18:19], v[22:23]\n"
    "v_pk_add_f32 v[24:25], v[24:25], v[28:29]\n"
    "v_pk_add_f32 v[26:27], v[26:27], v[30:31]\n"
    "v_pk_add_f32 v[32:33], v[16:17], v[24:25]\n"
    "v_pk_add_f32 v[34:35], v[18:19], v[26:27]\n"
    "ds_read_b128 v[16:19], %[la] offset:12288\n"
    "ds_read_b128 v[20:23], %[la] offset:47040\n"
    "ds_read_b128 v[24:27], v42 offset:12288\n"
    "ds_read_b128 v[28:31], v42 offset:47040\n"
    "v_pk_fma_f32 v[32:33], v[32:33], v[66:67], v[48:49]\n"
    "v_pk_fma_f32 v[34:35], v[34:35], v[66:67], v[50:51]\n"
    "v_cvt_pk_bf16_f32 v38, v32, v33\n"
    "v_cvt_pk_bf16_f32 v39, v34, v35\n"
    "global_store_dwordx2 v111, v[38:39], %[outp] offset:0\n"
    "s_waitcnt lgkmcnt(4)\n"
    "v_pk_add_f32 v[0:1], v[0:1], v[4:5]\n"
    "v_pk_add_f32 v[2:3], v[2:3], v[6:7]\n"
    "v_pk_add_f32 v[8:9], v[8:9], v[12:13]\n"
    "v_pk_add_f32 v[10:11], v[10:11], v[14:15]\n"
    "v_pk_add_f32 v[32:33], v[0:1], v[8:9]\n"
    "v_pk_add_f32 v[34:35], v[2:3], v[10:11]\n"
    "ds_read_b128 v[0:3], %[la] offset:16384\n"
    "ds_read_b128 v[4:7], %[la] offset:51136\n"
    "ds_read_b128 v[8:11], v42 offset:16384\n"
    "ds_read_b128 v[12:15], v42 offset:51136\n"
    "v_pk_fma_f32 v[32:33], v[32:33], v[64:65], v[52:53]\n"
    "v_pk_fma_f32 v[34:35], v[34:35], v[64:65], v[54:55]\n"
    "v_cvt_pk_bf16_f32 v36, v32, v33\n"
    "v_cvt_pk_bf16_f32 v37, v34, v35\n"
    "global_store_dwordx2 v110, v[36:37], %[outp] offset:64\n"
    "s_waitcnt lgkmcnt(4)\n"
    "v_pk_add_f32 v[16:17], v[16:17], v[20:21]\n"
    "v_pk_add_f32 v[18:19], v[18:19], v[22:23]\n"
    "v_pk_add_f32 v[24:25], v[24:25], v[28:29]\n"
    "v_pk_add_f32 v[26:27], v[26:27], v[30:31]\n"
    "v_pk_add_f32 v[32:33], v[16:17], v[24:25]\n"
    "v_pk_add_f32 v[34:35], v[18:19], v[26:27]\n"
    "v_pk_fma_f32 v[32:33], v[32:33], v[66:67], v[56:57]\n"
    "v_pk_fma_f32 v[34:35], v[34:35], v[66:67], v[58:59]\n"
    "v_cvt_pk_bf16_f32 v38, v32, v33\n"
    "v_cvt_pk_bf16_f32 v39, v34, v35\n"
    "global_store_dwordx2 v111, v[38:39], %[outp] offset:64\n"
    "s_waitcnt lgkmcnt(0)\n"
    "v_pk_add_f32 v[0:1], v[0:1], v[4:5]\n"
    "v_pk_add_f32 v[2:3], v[2:3], v[6:7]\n"
    "v_pk_add_f32 v[8:9], v[8:9], v[12:13]\n"
    "v_pk_add_f32 v[10:11], v[10:11], v[14:15]\n"
    "v_pk_add_f32 v[32:33], v[0:1], v[8:9]\n"
    "v_pk_add_f32 v[34:35], v[2:3], v[10:11]\n"
    "v_pk_fma_f32 v[32:33], v[32:33], v[64:65], v[60:61]\n"
    "v_pk_fma_f32 v[34:35], v[34:35], v[64:65], v[62:63]\n"
    "v_cvt_pk_bf16_f32 v36, v32, v33\n"
    "v_cvt_pk_bf16_f32 v37, v34, v35\n"
    "global_store_dwordx2 v112, v[36:37], %[outp] offset:0\n"
      : : "{v[100:107]}"(xres0), "{v[108:109]}"(xres1), "{v[110:113]}"(a110), [ra] "v"(L0 + L_RS + (ty * 32 + rr) * 4), [rb] "v"(L0 + L_RS + ((1 - ty) * 32 + rr) * 4),
         [la] "v"(L0 + (cg >> 1) * 1024 + (((cg & 1) * 32 + (rr ^ (cg << 1))) << 4)), [ps2] "s"(2 * PS), [outp] "s"(out), [flb] "s"(L0 + L_FL)
      : "v0", "v1", "v2", "v3", "v4", "v5", "v6", "v7", "v8", "v9", "v10", "v11", "v12", "v13", "v14", "v15", "v16", "v17", "v18", "v19", "v20", "v21", "v22", "v23", "v24", "v25", "v26", "v27", "v28", "v29", "v30", "v31", "v32", "v33", "v34", "v35", "v36", "v37", "v38", "v39", "v40", "v41", "v42", "v43", "v44", "v45", "v46", "v47", "v48", "v49", "v50", "v51", "v52", "v53", "v54", "v55", "v56", "v57", "v58", "v59", "v60", "v61", "v62", "v63", "v64", "v65", "v66", "v67", "v68", "s60", "s61", "scc", "memory");
    if (b == 0 && tid == 0) st8(ws + W_NONCE, epoch);
    __builtin_amdgcn_endpgm();
  }
}

extern "C" void kernel_launch(void* const* d_in, const int* in_sizes, int n_in, void* d_out, int out_size, void* d_ws, size_t ws_size, hipStream_t stream) {
  fused_block_kernel<<<256, 512, 0, stream>>>((const u16*)d_in[0], (const u16*)d_in[1], (const u16*)d_in[2], (const u16*)d_in[3], (const u16*)d_in[4],
                                              (const u16*)d_in[6], (const u16*)d_in[7], (u16*)d_out, (char*)d_ws);
}
```
